```python
import torch

def _softmax(x, axis=-1):
    return torch.softmax(x, dim=axis)
def _gelu(x):
    return torch.nn.functional.gelu(x, approximate="tanh")
def _silu(x):
    return torch.nn.functional.silu(x)
def _sigmoid(x):
    return torch.sigmoid(x)
def _layernorm(x, axis=-1, eps=1e-5):
    m = x.mean(dim=axis, keepdim=True)
    v = x.var(dim=axis, keepdim=True, unbiased=False)
    return (x - m) / torch.sqrt(v + eps)
def _scatter_add(tgt, idx, src, axis=0):
    return tgt.clone().index_add_(axis, idx, src)
def _segment_sum(src, seg, n):
    out = torch.zeros((n,) + src.shape[1:], dtype=src.dtype)
    return out.index_add_(0, seg, src)
def _pad(x, pw):
    flat = tuple(v for pair in reversed(pw) for v in pair)
    return torch.nn.functional.pad(x, flat)

kernel_name = 'recurrent_scan__spectral_fnet__moe_arch_mod__prompt'

def setup_inputs():

    _w = (lambda a: (lambda t: t.to(torch.bfloat16) if t.is_floating_point() else t)(torch.as_tensor(a)))
    def _A_setup():
        B, S, D = 1, 2048, 640
        ns = torch.arange(S)[:, None] * torch.arange(S)[None, :]
        nd = torch.arange(D)[:, None] * torch.arange(D)[None, :]
        Ws_re = torch.cos(-2.0 * torch.pi * ns / S).to(float)
        Ws_im = torch.sin(-2.0 * torch.pi * ns / S).to(float)
        Wd_re = torch.cos(-2.0 * torch.pi * nd / D).to(float)
        Wd_im = torch.sin(-2.0 * torch.pi * nd / D).to(float)
        return {
            "x": _w((lambda *s: torch.randn(*s, dtype=torch.bfloat16) * 0.1)(B, S, D)),
            "dft_seq_re": _w(Ws_re),
            "dft_seq_im": _w(Ws_im),
            "dft_hid_re": _w(Wd_re),
            "dft_hid_im": _w(Wd_im),
        }
    _A_in = _A_setup()

    def _B_setup():
        B, S, D, F, C = 1, 2048, 640, 1728, 2
        return {
            "x": _w((lambda *s: torch.randn(*s, dtype=torch.bfloat16) * 0.1)(B, S, D)),
            "w_r": _w((lambda *s: torch.randn(*s, dtype=torch.bfloat16) * 0.1)(D)),
            "w_gate": _w((lambda *s: torch.randn(*s, dtype=torch.bfloat16) * 0.1)(D, F)),
            "w_up": _w((lambda *s: torch.randn(*s, dtype=torch.bfloat16) * 0.1)(D, F)),
            "w_down": _w((lambda *s: torch.randn(*s, dtype=torch.bfloat16) * 0.1)(F, D)),
        }
    _B_in = _B_setup()

    return {
        "A_x": _A_in["x"],
        "A_dft_seq_re": _A_in["dft_seq_re"],
        "A_dft_seq_im": _A_in["dft_seq_im"],
        "A_dft_hid_re": _A_in["dft_hid_re"],
        "A_dft_hid_im": _A_in["dft_hid_im"],
        "B_x": _B_in["x"],
        "B_w_r": _B_in["w_r"],
        "B_w_gate": _B_in["w_gate"],
        "B_w_up": _B_in["w_up"],
        "B_w_down": _B_in["w_down"],
    }

def reference(A_x, A_dft_seq_re, A_dft_seq_im, A_dft_hid_re, A_dft_hid_im, B_x, B_w_r, B_w_gate, B_w_up, B_w_down):
    def _A_ref(x, dft_seq_re, dft_seq_im, dft_hid_re, dft_hid_im):
        a = torch.einsum("bsd,ed->bse", x, dft_hid_re)
        b = torch.einsum("bsd,ed->bse", x, dft_hid_im)
        y = torch.einsum("ts,bse->bte", dft_seq_re, a) - torch.einsum("ts,bse->bte", dft_seq_im, b)
        return y

    def _B_ref(x, w_r, w_gate, w_up, w_down):
        B, S, D = x.shape
        C = 2
        r = x @ w_r
        idx = torch.argsort(-r, axis=-1)[:, :C]
        idx_e = (lambda x, n, axis: torch.repeat_interleave(x, n, dim=axis))(idx.reshape(B, C, 1), D, axis=2)
        x_sel = (lambda x, i, axis: torch.take_along_dim(x, i, dim=axis))(x, idx_e, axis=1)
        g = _silu(x_sel @ w_gate)
        u = x_sel @ w_up
        y = (g * u) @ w_down
        r_sel = _sigmoid((lambda x, i, axis: torch.take_along_dim(x, i, dim=axis))(r, idx, axis=1))
        y = y * r_sel.reshape(B, C, 1)
        b_off = (torch.arange(B) * S).reshape(B, 1)
        flat_idx = (idx + b_off).reshape(B * C)
        zero = (x * 0.0).reshape(B * S, D)
        upd = _scatter_add(zero, flat_idx, y.reshape(B * C, D), axis=0)
        return x + upd.reshape(B, S, D)

    x = A_x
    xn = _layernorm(x, axis=-1)
    s = _A_ref(xn, A_dft_seq_re, A_dft_seq_im, A_dft_hid_re, A_dft_hid_im)
    g = _silu(_B_ref(xn, B_w_r, B_w_gate, B_w_up, B_w_down))
    return x + s * g
```

```cpp
#include <hip/hip_runtime.h>
#include <hip/hip_bf16.h>
#include <stdint.h>
typedef unsigned short u16;
constexpr int S=2048, D=640, F=1728;
__device__ __forceinline__ float bf2f(u16 v){ return __uint_as_float(((unsigned)v)<<16); }
__device__ __forceinline__ u16 f2bf(float f){ __hip_bfloat16 h = __float2bfloat16(f); return *(u16*)&h; }
__device__ __forceinline__ float rbf(float f){ return bf2f(f2bf(f)); }

__global__ void k_ln(const u16* x, const u16* wr, u16* xn, float* r){
  int row=blockIdx.x; int l=threadIdx.x;
  float v[10]; float s=0;
  for(int i=0;i<10;i++){ v[i]=bf2f(x[row*D+i*64+l]); s+=v[i]; }
  for(int o=32;o;o>>=1) s+=__shfl_xor(s,o);
  float m=s/D; float q=0;
  for(int i=0;i<10;i++){ float d=v[i]-m; q+=d*d; }
  for(int o=32;o;o>>=1) q+=__shfl_xor(q,o);
  float var=rbf(q/D); float mb=rbf(m);
  float den=rbf(sqrtf(rbf(var+1e-5f)));
  float rr=0;
  for(int i=0;i<10;i++){ float t=rbf(rbf(v[i]-mb)/den); xn[row*D+i*64+l]=f2bf(t); rr+=t*bf2f(wr[i*64+l]); }
  for(int o=32;o;o>>=1) rr+=__shfl_xor(rr,o);
  if(l==0) r[row]=rbf(rr);
}
__global__ void k_g1(const u16* Cr,const u16* Ci,const u16* xn,u16* PQ){
  int e=blockIdx.x*64+threadIdx.x; int t=blockIdx.y; int w=blockIdx.z;
  const u16* C=(w?Ci:Cr)+(size_t)t*S;
  float acc=0;
  for(int s=0;s<S;s++) acc+=bf2f(C[s])*bf2f(xn[s*D+e]);
  PQ[t*1280+w*640+e]=f2bf(acc);
}
__global__ void k_top(const float* r,int* idx){
  if(threadIdx.x==0){ int i0=0; for(int i=1;i<S;i++) if(r[i]>r[i0]) i0=i;
    int i1=(i0==0)?1:0; for(int i=0;i<S;i++) if(i!=i0 && r[i]>r[i1]) i1=i;
    idx[0]=i0; idx[1]=i1; }
}
__global__ void k_h(const u16* xn,const int* idx,const u16* wg,const u16* wu,float* h){
  int f=blockIdx.x*64+threadIdx.x; int c=blockIdx.y; const u16* xr=xn+idx[c]*D;
  float g=0,u=0;
  for(int d=0;d<D;d++){ float xv=bf2f(xr[d]); g+=xv*bf2f(wg[d*F+f]); u+=xv*bf2f(wu[d*F+f]); }
  g=rbf(g); u=rbf(u); g=rbf(g/(1.f+__expf(-g)));
  h[c*F+f]=rbf(g*u);
}
__global__ void k_y(const float* h,const int* idx,const float* r,const u16* wd,float* y){
  int d=blockIdx.x*64+threadIdx.x; int c=blockIdx.y;
  float a=0; for(int f=0;f<F;f++) a+=h[c*F+f]*bf2f(wd[f*D+d]);
  float rs=rbf(1.f/(1.f+__expf(-r[idx[c]])));
  y[c*D+d]=rbf(rbf(a)*rs);
}
__global__ void k_g2(const u16* PQ,const u16* Dr,const u16* Di,const u16* x,const u16* xn,const int* idx,const float* y,u16* out){
  int e=blockIdx.x*64+threadIdx.x; int t=blockIdx.y;
  float acc=0;
  for(int d=0;d<D;d++) acc+=bf2f(PQ[t*1280+d])*bf2f(Dr[e*D+d])-bf2f(PQ[t*1280+640+d])*bf2f(Di[e*D+d]);
  float s=rbf(acc);
  float z=bf2f(xn[t*D+e]);
  if(t==idx[0]) z=rbf(z+y[e]); else if(t==idx[1]) z=rbf(z+y[D+e]);
  float g=rbf(z/(1.f+__expf(-z)));
  out[t*D+e]=f2bf(bf2f(x[t*D+e])+rbf(s*g));
}
extern "C" void kernel_launch(void* const* d_in, const int* in_sizes, int n_in,
                              void* d_out, int out_size, void* d_ws, size_t ws_size,
                              hipStream_t stream) {
  const u16* x=(const u16*)d_in[0]; const u16* Cr=(const u16*)d_in[1]; const u16* Ci=(const u16*)d_in[2];
  const u16* Dr=(const u16*)d_in[3]; const u16* Di=(const u16*)d_in[4];
  const u16* wr=(const u16*)d_in[6]; const u16* wg=(const u16*)d_in[7]; const u16* wu=(const u16*)d_in[8]; const u16* wd=(const u16*)d_in[9];
  char* ws=(char*)d_ws;
  u16* xn=(u16*)ws; u16* PQ=(u16*)(ws+(4<<20)); float* r=(float*)(ws+(12<<20)); int* idx=(int*)(ws+(13<<20));
  float* h=(float*)(ws+(14<<20)); float* y=(float*)(ws+(15<<20));
  k_ln<<<S,64,0,stream>>>(x,wr,xn,r);
  k_g1<<<dim3(10,S,2),64,0,stream>>>(Cr,Ci,xn,PQ);
  k_top<<<1,64,0,stream>>>(r,idx);
  k_h<<<dim3(27,2),64,0,stream>>>(xn,idx,wg,wu,h);
  k_y<<<dim3(10,2),64,0,stream>>>(h,idx,r,wd,y);
  k_g2<<<dim3(10,S),64,0,stream>>>(PQ,Dr,Di,x,xn,idx,y,(u16*)d_out);
}
```

```cpp
#define STAT 0
#include <hip/hip_runtime.h>
#include <hip/hip_bf16.h>
#include <stdint.h>
typedef unsigned short u16; typedef unsigned u32;
typedef __attribute__((ext_vector_type(8))) short bf16x8;
typedef __attribute__((ext_vector_type(4))) float f32x4;
typedef __attribute__((ext_vector_type(4))) unsigned u32x4;
typedef __attribute__((ext_vector_type(2))) unsigned u32x2;
typedef __attribute__((ext_vector_type(4))) int i32x4;
constexpr int S=2048, D=640, F=1728;
__device__ __forceinline__ float bf2f(u16 v){ return __uint_as_float(((u32)v)<<16); }
__device__ __forceinline__ u16 f2bf(float f){ __hip_bfloat16 h = __float2bfloat16(f); return *(u16*)&h; }
__device__ __forceinline__ float rbf(float f){ return bf2f(f2bf(f)); }
__device__ __forceinline__ u32 pk2(float a,float b){ return (u32)f2bf(a)|((u32)f2bf(b)<<16); }
__device__ __forceinline__ i32x4 mk_srd(const void* p, u32 bytes){
  unsigned long long a=(unsigned long long)p; i32x4 r;
  r[0]=__builtin_amdgcn_readfirstlane((u32)a); r[1]=__builtin_amdgcn_readfirstlane((u32)(a>>32))&0xffff; r[2]=bytes; r[3]=0x00020000; return r;
}
#define POL ""
__device__ __forceinline__ void bl_lds(i32x4 srd, u32 voff, u32 soff, u32 ldsaddr){
  asm volatile("s_mov_b32 m0, %3\n\ts_nop 0\n\tbuffer_load_dwordx4 %0, %1, %2 offen lds" :: "v"(voff), "s"(srd), "s"(soff), "s"(ldsaddr) : "memory");
}
__device__ __forceinline__ void bl_lds_p(i32x4 srd, u32 voff, u32 soff, u32 ldsaddr){
  asm volatile("s_mov_b32 m0, %3\n\ts_nop 0\n\tbuffer_load_dwordx4 %0, %1, %2 offen " POL " lds" :: "v"(voff), "s"(srd), "s"(soff), "s"(ldsaddr) : "memory");
}
template<int N> __device__ __forceinline__ void waitv(){ asm volatile("s_waitcnt vmcnt(%0)"::"n"(N):"memory"); }
#define BAR() do{ asm volatile("s_waitcnt lgkmcnt(0)":::"memory"); __builtin_amdgcn_s_barrier(); asm volatile("":::"memory"); }while(0)

__global__ __launch_bounds__(256) void k_ln(const u16* x, const u16* wr, u16* xn, float* r){
  int row=blockIdx.x*4+(threadIdx.x>>6); int l=threadIdx.x&63;
  const u32* xp=(const u32*)(x+row*D); const u32* wp=(const u32*)wr;
  float v[10]; float s=0;
  #pragma unroll
  for(int i=0;i<5;i++){ u32 t=xp[i*64+l]; v[2*i]=__uint_as_float(t<<16); v[2*i+1]=__uint_as_float(t&0xffff0000u); s+=v[2*i]+v[2*i+1]; }
  for(int o=32;o;o>>=1) s+=__shfl_xor(s,o);
  float m=s*(1.f/D); float q=0;
  #pragma unroll
  for(int i=0;i<10;i++){ float d=v[i]-m; q+=d*d; }
  for(int o=32;o;o>>=1) q+=__shfl_xor(q,o);
  float var=rbf(q*(1.f/D)); float mb=rbf(m);
  float den=rbf(sqrtf(rbf(var+1e-5f)));
  float rr=0;
  #pragma unroll
  for(int i=0;i<5;i++){ float t0=rbf(rbf(v[2*i]-mb)/den), t1=rbf(rbf(v[2*i+1]-mb)/den); u32 w=wp[i*64+l];
    ((u32*)(xn+row*D))[i*64+l]=pk2(t0,t1); rr+=t0*__uint_as_float(w<<16)+t1*__uint_as_float(w&0xffff0000u); }
  for(int o=32;o;o>>=1) rr+=__shfl_xor(rr,o);
  if(l==0) r[row]=rbf(rr);
}
__device__ __forceinline__ void top2(const float* r,int* sh,int& i0,int& i1){
  int t=threadIdx.x; u32 k1=0,k2=0;
  #pragma unroll
  for(int i=0;i<8;i++){ int idx=i*256+t; u32 b=__float_as_uint(r[idx])>>16; b=(b&0x8000)?(~b&0xffff):(b|0x8000); u32 k=(b<<11)|(2047-idx);
    u32 lo=min(k,k1); k1=max(k,k1); k2=max(k2,lo); }
  for(int o=32;o;o>>=1){ u32 a=__shfl_xor(k1,o), b=__shfl_xor(k2,o); u32 lo=min(a,k1); k1=max(a,k1); k2=max(max(k2,b),lo); }
  if((t&63)==0){ sh[(t>>6)*2]=k1; sh[(t>>6)*2+1]=k2; }
  __syncthreads();
  k1=0;k2=0;
  for(int i=0;i<8;i++){ u32 k=sh[i]; u32 lo=min(k,k1); k1=max(k,k1); k2=max(k2,lo); }
  i0=2047-(k1&2047); i1=2047-(k2&2047);
  __syncthreads();
}
__global__ __launch_bounds__(256) void k_g1(const u16* xn,const u16* Dr,const u16* Di,u16* abF,const float* r,const u16* wg,const u16* wu,const u16* wd,float* ypart,int* idxo){
  extern __shared__ __attribute__((aligned(1024))) char lds[];
  const int b=blockIdx.x; const int lane=threadIdx.x&63, w=__builtin_amdgcn_readfirstlane(threadIdx.x>>6);
  if(b>=256){
    int fb=b-256; int i0,i1; int* sh=(int*)lds; top2(r,sh,i0,i1);
    if(fb==0&&threadIdx.x==0){ idxo[0]=i0; idxo[1]=i1; }
    float* xs=(float*)(lds+64); float* red=(float*)(lds+64+5120); float* hs=(float*)(lds+64+5120+32768);
    for(int i=threadIdx.x;i<D;i+=256){ xs[i]=bf2f(xn[i0*D+i]); xs[D+i]=bf2f(xn[i1*D+i]); }
    __syncthreads();
    int cq=threadIdx.x&3, dr=threadIdx.x>>2;
    float g0[8]={},g1[8]={},u0[8]={},u1[8]={};
    #pragma unroll
    for(int i=0;i<10;i++){ int d=i*64+dr; u32x4 a=*(const u32x4*)(wg+(size_t)d*F+fb*32+cq*8), c=*(const u32x4*)(wu+(size_t)d*F+fb*32+cq*8); float x0=xs[d],x1=xs[D+d];
      #pragma unroll
      for(int j=0;j<4;j++){ float al=__uint_as_float(a[j]<<16),ah=__uint_as_float(a[j]&0xffff0000u),cl=__uint_as_float(c[j]<<16),ch=__uint_as_float(c[j]&0xffff0000u);
        g0[2*j]+=x0*al; g0[2*j+1]+=x0*ah; g1[2*j]+=x1*al; g1[2*j+1]+=x1*ah; u0[2*j]+=x0*cl; u0[2*j+1]+=x0*ch; u1[2*j]+=x1*cl; u1[2*j+1]+=x1*ch; } }
    #pragma unroll
    for(int j=0;j<8;j++){ red[(0*32+cq*8+j)*64+dr]=g0[j]; red[(1*32+cq*8+j)*64+dr]=g1[j]; red[(2*32+cq*8+j)*64+dr]=u0[j]; red[(3*32+cq*8+j)*64+dr]=u1[j]; }
    __syncthreads();
    if(threadIdx.x<64){ int c=threadIdx.x>>5,f=threadIdx.x&31; float g=0,u=0; for(int i=0;i<64;i++){ int ii=(i+threadIdx.x)&63; g+=red[(c*32+f)*64+ii]; u+=red[((2+c)*32+f)*64+ii]; }
      g=rbf(g); u=rbf(u); g=rbf(g/(1.f+__expf(-g))); hs[c*32+f]=rbf(g*u); }
    __syncthreads();
    if(threadIdx.x<160){ int e=threadIdx.x*4; float y0[4]={},y1[4]={};
      #pragma unroll 8
      for(int f=0;f<32;f++){ u32x2 a=*(const u32x2*)(wd+(size_t)(fb*32+f)*D+e); float h0=hs[f],h1=hs[32+f];
        float w0=__uint_as_float(a[0]<<16),w1=__uint_as_float(a[0]&0xffff0000u),w2=__uint_as_float(a[1]<<16),w3=__uint_as_float(a[1]&0xffff0000u);
        y0[0]+=h0*w0;y0[1]+=h0*w1;y0[2]+=h0*w2;y0[3]+=h0*w3; y1[0]+=h1*w0;y1[1]+=h1*w1;y1[2]+=h1*w2;y1[3]+=h1*w3; }
      *(f32x4*)(ypart+(fb*2+0)*D+e)=f32x4{y0[0],y0[1],y0[2],y0[3]}; *(f32x4*)(ypart+(fb*2+1)*D+e)=f32x4{y1[0],y1[1],y1[2],y1[3]}; }
    return;
  }
  const int xcd=b&7, j=b>>3, h=xcd&1, rbp=(xcd>>1)*4+(j>>3), nt=j&7;
  i32x4 sa=mk_srd(xn,S*D*2), sb=mk_srd(h?Di:Dr,D*D*2);
  u32 ldsbase=__builtin_amdgcn_readfirstlane((u32)(size_t)lds);
  u32 r8=lane>>3, ch=lane&7;
  u32 v0=r8*1280u+((ch^((r8>>1)&7))<<4), v1=r8*1280u+((ch^(((r8+8)>>1)&7))<<4);
  constexpr int NS=3, SLOT=26*1024+2048;
  auto stage=[&](int ks){ u32 slot=ldsbase+(ks%NS)*SLOT;
    #pragma unroll
    for(int p=0;p<4;p++) bl_lds(sa,(p&1)?v1:v0,(u32)(rbp*128+w*32+p*8)*1280u+ks*128u,slot+(w*4+p)*1024);
    #pragma unroll
    for(int p=0;p<3;p++){ int q=p*4+w; int qq=q<10?q:q-2; bl_lds(sb,(w&1)?v1:v0,(u32)(nt*80+qq*8)*1280u+ks*128u,slot+(16+q)*1024); } };
  f32x4 acc[2][5]={};
  int fr=lane&15,fq=lane>>4;
  int ar0=8*(fr>>2)+(fr&3), ar1=ar0+4;
  stage(0); stage(1);
  for(int ks=0;ks<10;ks++){
    if(ks<9) waitv<7>(); else waitv<0>();
    BAR();
    if(ks+2<10) stage(ks+2);
    const char* sl=lds+(ks%NS)*SLOT;
    #pragma unroll
    for(int kk=0;kk<2;kk++){
      bf16x8 a0=*(const bf16x8*)(sl+(w*32+ar0)*128+(((kk*4+fq)^((ar0>>1)&7))<<4));
      bf16x8 a1=*(const bf16x8*)(sl+(w*32+ar1)*128+(((kk*4+fq)^((ar1>>1)&7))<<4));
      #pragma unroll
      for(int n=0;n<5;n++){ bf16x8 bb=*(const bf16x8*)(sl+16384+(n*16+fr)*128+(((kk*4+fq)^((fr>>1)&7))<<4));
        acc[0][n]=__builtin_amdgcn_mfma_f32_16x16x32_bf16(a0,bb,acc[0][n],0,0,0);
        acc[1][n]=__builtin_amdgcn_mfma_f32_16x16x32_bf16(a1,bb,acc[1][n],0,0,0); }
    }
  }
  float sg=h?-1.f:1.f;
  #pragma unroll
  for(int n=0;n<5;n++){ u32x4 o={pk2(sg*acc[0][n][0],sg*acc[0][n][1]),pk2(sg*acc[0][n][2],sg*acc[0][n][3]),pk2(sg*acc[1][n][0],sg*acc[1][n][1]),pk2(sg*acc[1][n][2],sg*acc[1][n][3])};
    *(u32x4*)((char*)abF+((size_t)((h*64+rbp*4+w)*40+nt*5+n)*64+lane)*16)=o; }
}
__global__ __launch_bounds__(256) void k_g2(const u16* Cr,const u16* Ci,const u16* abF,const u16* x,const u16* xn,const float* r,const float* ypart,const int* idx,u16* out){
  extern __shared__ __attribute__((aligned(1024))) char lds[];
  const int b=blockIdx.x; const int lane=threadIdx.x&63, w=__builtin_amdgcn_readfirstlane(threadIdx.x>>6);
  const int xcd=b&7, j=b>>3, rb=xcd*4+(j>>3), nt=j&7;
  i32x4 sr=mk_srd(Cr,S*S*2), si=mk_srd(Ci,S*S*2), sb=mk_srd(abF,2*S*D*2);
  u32 ldsbase=__builtin_amdgcn_readfirstlane((u32)(size_t)lds);
  u32 r8=lane>>3, ch=lane&7;
  u32 v0=r8*4096u+((ch^((r8>>1)&7))<<4), v1=r8*4096u+((ch^(((r8+8)>>1)&7))<<4), vB=lane*16;
  constexpr int NS=6, SLOT=20*1024;
#define ROT 8
  auto stage=[&](int it){ u32 slot=ldsbase+(it%NS)*SLOT; int ks=(it+nt*ROT)&63;
    i32x4 sa=(ks<32)?sr:si;
    #pragma unroll
    for(int p=0;p<2;p++) bl_lds_p(sa,p?v1:v0,(u32)(rb*64+w*16+p*8)*4096u+(ks&31)*128u,slot+(w*2+p)*1024);
    #pragma unroll
    for(int p=0;p<3;p++){ int q=p*4+w; int qq=q<10?q:q-2; int kk=qq>=5; bl_lds(sb,vB,(u32)((ks*2+kk)*40+nt*5+qq-kk*5)*1024u,slot+(8+q)*1024); } };
  f32x4 acc[2][5]={};
  int fr=lane&15,fq=lane>>4, mh=w&1, kk=w>>1;
  for(int i=0;i<NS-1;i++) stage(i);
  for(int it=0;it<64;it++){
    if(it<64-(NS-2)) waitv<5*(NS-2)>(); else waitv<0>();
    BAR();
    if(it+NS-1<64) stage(it+NS-1);
    const char* sl=lds+(it%NS)*SLOT;
    bf16x8 a0=*(const bf16x8*)(sl+(mh*32+fr)*128+(((kk*4+fq)^((fr>>1)&7))<<4));
    bf16x8 a1=*(const bf16x8*)(sl+(mh*32+16+fr)*128+(((kk*4+fq)^((fr>>1)&7))<<4));
    #pragma unroll
    for(int n=0;n<5;n++){ bf16x8 bb=*(const bf16x8*)(sl+8192+(kk*5+n)*1024+lane*16);
      acc[0][n]=__builtin_amdgcn_mfma_f32_16x16x32_bf16(bb,a0,acc[0][n],0,0,0);
      acc[1][n]=__builtin_amdgcn_mfma_f32_16x16x32_bf16(bb,a1,acc[1][n],0,0,0); }
  }
  BAR();
  f32x4* red=(f32x4*)lds;
  #pragma unroll
  for(int n=0;n<5;n++) red[(w*5+n)*64+lane]=kk?acc[0][n]:acc[1][n];
  __syncthreads();
  int t=rb*64+mh*32+kk*16+fr; int i0=idx[0],i1=idx[1];
  #pragma unroll
  for(int n=0;n<5;n++){ f32x4 o=red[((w^2)*5+n)*64+lane]; f32x4 s=(kk?acc[1][n]:acc[0][n])+o;
    int e=nt*80+n*16+fq*4;
    u32x2 xv=*(const u32x2*)(x+t*D+e), zv=*(const u32x2*)(xn+t*D+e);
    float xf[4]={__uint_as_float(xv[0]<<16),__uint_as_float(xv[0]&0xffff0000u),__uint_as_float(xv[1]<<16),__uint_as_float(xv[1]&0xffff0000u)};
    float z[4]={__uint_as_float(zv[0]<<16),__uint_as_float(zv[0]&0xffff0000u),__uint_as_float(zv[1]<<16),__uint_as_float(zv[1]&0xffff0000u)};
    if(t==i0||t==i1){ int c=(t==i1); f32x4 y={0,0,0,0};
      #pragma unroll 6
      for(int p=0;p<54;p++) y+=*(const f32x4*)(ypart+(p*2+c)*D+e);
      float rs=rbf(1.f/(1.f+__expf(-r[t])));
      #pragma unroll
      for(int q=0;q<4;q++) z[q]=rbf(z[q]+rbf(rbf(y[q])*rs)); }
    float o4[4];
    #pragma unroll
    for(int q=0;q<4;q++){ float g=rbf(z[q]/(1.f+__expf(-z[q]))); o4[q]=xf[q]+rbf(rbf(s[q])*g); }
    *(u32x2*)(out+t*D+e)=u32x2{pk2(o4[0],o4[1]),pk2(o4[2],o4[3])}; }
}
__global__ void k_empty(u32* p){ if(p==nullptr) p[0]=1; }

__global__ __launch_bounds__(256) void k_meas(const void* A,unsigned long long* ts){
  extern __shared__ __attribute__((aligned(1024))) char lds[];
  unsigned long long t0=__builtin_amdgcn_s_memrealtime();
  unsigned long long c0=__builtin_amdgcn_s_memtime();
  const int b=blockIdx.x; const int lane=threadIdx.x&63, w=__builtin_amdgcn_readfirstlane(threadIdx.x>>6);
  i32x4 sa=mk_srd(A,S*S*2);
  u32 ldsbase=__builtin_amdgcn_readfirstlane((u32)(size_t)lds);
  u32 vB=lane*16;
  #pragma unroll
  for(int p=0;p<8;p++) bl_lds(sa,vB,(u32)(b*32+w*8+p)*1024u,ldsbase+(w*8+p)*1024);
  waitv<7>();
  unsigned long long t1=__builtin_amdgcn_s_memrealtime();
  waitv<0>();
  __syncthreads();
  unsigned long long t2=__builtin_amdgcn_s_memrealtime();
  unsigned long long c2=__builtin_amdgcn_s_memtime();
  if(threadIdx.x==0){ ts[b*4]=t0; ts[b*4+1]=t1; ts[b*4+2]=t2; ts[b*4+3]=c2-c0; }
}
__global__ __launch_bounds__(64) void k_spin(const unsigned long long* ts){
  unsigned long long mn0=~0ull,mx0=0,mx2=0,s1=0,s2=0,sc=0,mx1=0;
  for(int i=0;i<256;i++){ unsigned long long a=ts[i*4],b=ts[i*4+1],c=ts[i*4+2]; mn0=a<mn0?a:mn0; mx0=a>mx0?a:mx0; mx2=c>mx2?c:mx2; s1+=b-a; s2+=c-a; sc+=ts[i*4+3]; mx1=(b-a)>mx1?(b-a):mx1; }
  unsigned long long stat=0;
  if(STAT==0) stat=0;
  if(STAT==1) stat=(mx0-mn0)*50;
  if(STAT==2) stat=s1*50/256;
  if(STAT==3) stat=(mx2-mn0)*50;
  if(STAT==4) stat=sc*500/s2;
  if(STAT==5) stat=s2*50/256;
  if(stat>100000) stat=100000;
  unsigned long long t0=__builtin_amdgcn_s_memrealtime();
  while(__builtin_amdgcn_s_memrealtime()-t0<stat) __builtin_amdgcn_s_sleep(1);
}

extern "C" void kernel_launch(void* const* d_in, const int* in_sizes, int n_in,
                              void* d_out, int out_size, void* d_ws, size_t ws_size,
                              hipStream_t stream) {
  const u16* x=(const u16*)d_in[0]; const u16* Cr=(const u16*)d_in[1]; const u16* Ci=(const u16*)d_in[2];
  const u16* Dr=(const u16*)d_in[3]; const u16* Di=(const u16*)d_in[4];
  const u16* wr=(const u16*)d_in[6]; const u16* wg=(const u16*)d_in[7]; const u16* wu=(const u16*)d_in[8]; const u16* wd=(const u16*)d_in[9];
  char* ws=(char*)d_ws;
  u16* xn=(u16*)ws; u16* abF=(u16*)(ws+(4<<20)); float* r=(float*)(ws+(12<<20)); int* idx=(int*)(ws+(13<<20)); float* yp=(float*)(ws+(14<<20));
  hipFuncSetAttribute((const void*)k_g1,hipFuncAttributeMaxDynamicSharedMemorySize,3*28*1024);
  hipFuncSetAttribute((const void*)k_g2,hipFuncAttributeMaxDynamicSharedMemorySize,6*20*1024);
  hipFuncSetAttribute((const void*)k_meas,hipFuncAttributeMaxDynamicSharedMemorySize,160*1024);
  k_meas<<<256,256,160*1024,stream>>>(Cr,(unsigned long long*)(ws+(20<<20)));
  k_spin<<<1,64,0,stream>>>((unsigned long long*)(ws+(20<<20)));
  k_ln<<<S/4,256,0,stream>>>(x,wr,xn,r);
  k_g1<<<256+54,256,3*28*1024,stream>>>(xn,Dr,Di,abF,r,wg,wu,wd,yp,idx);
  k_g2<<<256,256,6*20*1024,stream>>>(Cr,Ci,abF,x,xn,r,yp,idx,(u16*)d_out);
}
```

```cpp
#include <hip/hip_runtime.h>
#include <hip/hip_bf16.h>

__global__ __launch_bounds__(256, 1) void fnet_moe_kernel(const void* x, const void* cr, const void* ci, const void* dr, const void* di,
    const void* wr, const void* wg, const void* wu, const void* wd, void* ws, void* out) {
  asm volatile(
"Lpc_%=:\n"
"s_getpc_b64 s[6:7]\n"
"s_load_dwordx2 s[8:9], s[0:1], 0\n"
"s_and_b32 s43, s2, 1\n"
"s_lshl3_add_u32 s60, s43, 24\n"
"s_load_dwordx2 s[12:13], s[0:1], s60\n"
"s_load_dwordx2 s[36:37], s[0:1], 40\n"
"s_load_dwordx2 s[20:21], s[0:1], 72\n"
"s_and_b32 s61, s2, 0xf8\n"
"v_add_u32 v44, s61, v0\n"
"v_cmp_gt_u32 vcc, 8, v0\n"
"v_min_u32 v44, (Lend_%=-Lpc_%=)/128-1, v44\n"
"v_lshlrev_b32 v44, 7, v44\n"
"s_and_b64 exec, exec, vcc\n"
"s_cbranch_execz Lnopf_%=\n"
"global_load_dword v255, v44, s[6:7]\n"
"Lnopf_%=:\n"
"s_mov_b64 exec, -1\n"
"v_lshrrev_b32 v45, 6, v0\n"
"v_lshrrev_b32 v44, 5, v0\n"
"v_and_b32 v46, 31, v0\n"
"v_readfirstlane_b32 s40, v45\n"
"v_lshlrev_b32 v45, 4, v46\n"
"v_lshlrev_b32 v251, 3, v46\n"
"s_movk_i32 s67, 1280\n"
"v_mad_u32_u24 v46, v44, s67, v45\n"
"v_sub_u32 v250, v46, v251\n"
"s_lshr_b32 s41, s2, 3\n"
"s_bfe_u32 s46, s2, 0x20001\n"
"s_and_b32 s64, s2, 7\n"
"s_lshl_b32 s64, s64, 5\n"
"s_add_u32 s64, s64, s41\n"
"s_mul_i32 s65, s46, 20\n"
"s_add_u32 s65, s65, s41\n"
"s_mul_i32 s57, s64, 10240\n"
"s_mul_i32 s58, s65, 10240\n"
"s_cmp_lt_u32 s41, 20\n"
"s_cselect_b32 s58, s58, 1073741824\n"
"s_mov_b32 s10, 2621440\n"
"s_mov_b32 s11, 0x20000\n"
"s_mov_b32 s14, 819200\n"
"s_mov_b32 s15, 0x20000\n"
"s_movk_i32 s38, 1280\n"
"s_mov_b32 s39, 0x20000\n"
"v_bfe_u32 v47, v0, 3, 3\n"
"v_lshrrev_b32 v48, 1, v47\n"
"v_xor_b32 v48, v48, v0\n"
"v_and_b32 v48, 7, v48\n"
"v_lshlrev_b32 v48, 4, v48\n"
"v_and_b32 v49, 4, v47\n"
"v_add_u32 v49, v49, v47\n"
"v_mad_u32_u24 v48, v49, s67, v48\n"
"v_xor_b32 v49, 64, v48\n"
"v_add_u32 v49, 19456, v49\n"
"s_lshr_b32 s66, s41, 3\n"
"s_lshl2_add_u32 s66, s46, s66\n"
"s_lshl2_add_u32 s73, s66, s40\n"
"s_mul_i32 s66, s73, 40960\n"
"v_add_u32 v33, s66, v48\n"
"v_add_u32 v34, s66, v49\n"
"v_add_u32 v35, 3072, v33\n"
"v_add_u32 v36, 3072, v34\n"
"s_and_b32 s44, s41, 3\n"
"s_bfe_u32 s45, s41, 0x10002\n"
"s_waitcnt lgkmcnt(0)\n"
"buffer_load_dwordx4 v[200:203], v46, s[8:11], s57 offen\n"
"buffer_load_dwordx4 v[204:207], v46, s[8:11], s57 offen offset:512\n"
"buffer_load_dwordx2 v[208:209], v250, s[8:11], s57 offen offset:1024\n"
"s_lshr_b32 s62, s40, 1\n"
"s_and_b32 s63, s40, 1\n"
"s_mul_i32 s66, s44, 40\n"
"s_add_u32 s75, s66, s45\n"
"s_addk_i32 s75, 32\n"
"s_mulk_i32 s75, 5120\n"
"buffer_load_dwordx4 v[210:213], v45, s[36:39], 0 offen\n"
"s_lshl4_add_u32 s66, s45, s66\n"
"s_lshl3_add_u32 s66, s62, s66\n"
"s_add_u32 s66, s66, s63\n"
"s_mulk_i32 s66, 5120\n"
"v_add_u32 v37, s66, v48\n"
"v_add_u32 v38, s66, v49\n"
"buffer_load_dwordx4 v[214:217], v45, s[36:39], 0 offen offset:512\n"
"s_mul_i32 s66, s63, 20480\n"
"s_add_u32 s75, s75, s66\n"
"s_lshl_b32 s66, s63, 6\n"
"v_xor_b32 v39, s66, v48\n"
"v_add_u32 v39, s75, v39\n"
"s_lshr_b32 s66, s41, 2\n"
"buffer_load_dwordx2 v[218:219], v251, s[36:39], 0 offen offset:1024\n"
"s_lshl1_add_u32 s66, s44, s66\n"
"s_mulk_i32 s66, 160\n"
"s_and_b32 s79, s66, 0xff80\n"
"s_sub_u32 s66, s79, s67\n"
"s_min_u32 s79, s79, s66\n"
"s_lshr_b32 s59, s79, 7\n"
"buffer_load_dwordx4 v[220:223], v46, s[12:15], s58 offen\n"
"s_lshl_b32 s66, s62, 7\n"
"s_add_u32 s80, s79, s66\n"
"s_sub_u32 s66, s80, s67\n"
"s_min_u32 s80, s80, s66\n"
"s_lshl_b32 s66, s40, 10\n"
"s_add_u32 s70, s66, 122880\n"
"buffer_load_dwordx4 v[224:227], v46, s[12:15], s58 offen offset:512\n"
"s_lshl1_add_u32 s69, s66, 16384\n"
"s_lshl2_add_u32 s68, s66, 0\n"
"s_load_dword s3, s[20:21], 0\n"
"s_lshl3_add_u32 s66, s43, 8\n"
"s_load_dwordx2 s[16:17], s[0:1], s66\n"
"buffer_load_dwordx2 v[228:229], v250, s[12:15], s58 offen offset:1024\n"
"s_lshl3_add_u32 s66, s62, 48\n"
"s_load_dwordx2 s[24:25], s[0:1], s66\n"
"s_load_dwordx2 s[28:29], s[0:1], 64\n"
"s_load_dwordx2 s[32:33], s[0:1], 80\n"
"s_mov_b32 s18, 8388608\n"
"s_mov_b32 s19, 0x20000\n"
"s_add_u32 m0, s68, 0\n"
"s_nop 0\n"
"buffer_load_dwordx4 v33, s[8:11], s79 offen offset:0 lds\n"
"s_mov_b32 s22, 23068672\n"
"s_mov_b32 s23, 0x20000\n"
"s_mov_b32 s4, 0xffff0000\n"
"s_mov_b32 s5, 0x3f803f80\n"
"v_and_b32 v1, 63, v0\n"
"v_lshlrev_b32 v2, 4, v1\n"
"buffer_load_dwordx4 v34, s[8:11], s79 offen offset:1024 lds\n"
"v_and_b32 v47, 15, v0\n"
"v_lshrrev_b32 v48, 4, v1\n"
"v_lshrrev_b32 v49, 1, v47\n"
"v_xor_b32 v49, v49, v48\n"
"v_lshlrev_b32 v49, 4, v49\n"
"v_lshl_add_u32 v49, v47, 7, v49\n"
"buffer_load_dwordx4 v35, s[8:11], s79 offen offset:2048 lds\n"
"v_add_u32 v40, s68, v49\n"
"v_xor_b32 v41, 64, v40\n"
"v_add_u32 v42, 16384, v49\n"
"v_xor_b32 v43, 64, v42\n"
"v_add_u32 v111, 73728, v40\n"
"v_add_u32 v112, 73728, v41\n"
"buffer_load_dwordx4 v36, s[8:11], s79 offen offset:3072 lds\n"
"v_add_u32 v113, 73728, v42\n"
"v_add_u32 v114, 73728, v43\n"
"s_lshr_b32 s61, s41, 2\n"
"s_lshl1_add_u32 s60, s45, s43\n"
"s_and_b32 s78, s73, 0xfc\n"
"s_add_u32 s78, s78, s60\n"
"s_add_u32 m0, s69, 0\n"
"s_nop 0\n"
"buffer_load_dwordx4 v37, s[12:15], s79 offen offset:0 lds\n"
"s_and_b32 s66, s78, 15\n"
"s_lshl2_add_u32 s66, s66, s40\n"
"s_lshl_b32 s66, s66, 5\n"
"s_add_u32 s66, s66, s46\n"
"s_lshl_b32 s50, s66, 10\n"
"s_lshl_b32 s66, s44, 2\n"
"s_lshr_b32 s66, 0x321321, s66\n"
"s_and_b32 s66, s66, 0xfff\n"
"s_mul_i32 s49, s46, 0x1111\n"
"s_xor_b32 s49, s49, s66\n"
"s_and_b32 s66, s61, 3\n"
"s_mul_i32 s53, s66, 65536\n"
"s_lshr_b32 s66, s53, 8\n"
"v_add_u32 v3, s66, v2\n"
"v_and_b32 v3, 0x3ff, v3\n"
"v_add_u32 v4, 3040, v3\n"
"v_add_u32 v5, 6080, v3\n"
"v_add_u32 v6, 9120, v3\n"
"s_mul_i32 s54, s40, 8448\n"
"s_addk_i32 s54, 128\n"
"s_mul_i32 s66, s60, 1536\n"
"s_lshl_b32 s61, s63, 6\n"
"s_add_u32 s66, s66, s61\n"
"s_add_u32 s66, s66, 156672\n"
"v_lshl_add_u32 v108, v47, 2, s66\n"
"v_lshrrev_b32 v50, 1, v47\n"
"v_and_b32 v50, 4, v50\n"
"v_and_b32 v51, 3, v0\n"
"s_lshl_b32 s66, s63, 3\n"
"v_add3_u32 v50, v50, v51, s66\n"
"v_lshrrev_b32 v51, 1, v50\n"
"v_xor_b32 v51, v51, v48\n"
"v_lshlrev_b32 v51, 4, v51\n"
"v_lshl_add_u32 v50, v50, 7, v51\n"
"v_and_b32 v51, 4, v0\n"
"v_lshl_add_u32 v50, v51, 9, v50\n"
"s_lshl_b32 s60, s60, 12\n"
"s_mul_i32 s61, s44, 5\n"
"s_add_u32 s61, s61, s40\n"
"s_lshr_b32 s55, s61, 1\n"
"s_sub_u32 s55, s55, s59\n"
"s_add_u32 s66, s55, 10\n"
"s_min_u32 s55, s55, s66\n"
"s_and_b32 s61, s61, 1\n"
"s_lshl_b32 s61, s61, 6\n"
"v_xor_b32 v105, s61, v50\n"
"s_sub_u32 s66, s55, 5\n"
"s_min_u32 s66, s66, s55\n"
"s_mulk_i32 s66, 24576\n"
"s_add_u32 s66, s66, s60\n"
"v_add_u32 v105, s66, v105\n"
"s_lshr_b32 s55, s55, 0\n"
"s_mul_i32 s61, s44, 5\n"
"s_add_u32 s61, s61, 4\n"
"s_lshr_b32 s56, s61, 1\n"
"s_sub_u32 s56, s56, s59\n"
"s_add_u32 s66, s56, 10\n"
"s_min_u32 s56, s56, s66\n"
"s_and_b32 s61, s61, 1\n"
"s_lshl_b32 s61, s61, 6\n"
"v_xor_b32 v107, s61, v50\n"
"s_sub_u32 s66, s56, 5\n"
"s_min_u32 s66, s66, s56\n"
"s_mulk_i32 s66, 24576\n"
"s_add_u32 s66, s66, s60\n"
"s_lshl3_add_u32 s66, s62, s66\n"
"v_add_u32 v107, s66, v107\n"
"s_lshr_b32 s56, s56, 0\n"
"v_xor_b32 v106, 1088, v105\n"
"s_bfe_u32 s48, s41, 0x20003\n"
"s_mov_b32 s26, 2211840\n"
"s_mov_b32 s27, 0x20000\n"
"s_mov_b32 s30, 2211840\n"
"s_mov_b32 s31, 0x20000\n"
"s_and_b32 s66, s2, 7\n"
"s_and_b32 s61, s41, 7\n"
"s_cmp_lt_u32 s41, 24\n"
"s_cselect_b32 s71, s66, s61\n"
"s_cselect_b32 s72, s61, s66\n"
"s_and_b32 s66, s41, 24\n"
"s_add_u32 s71, s71, s66\n"
"s_lshl1_add_u32 s66, s72, s63\n"
"s_mul_i32 s66, s66, 138240\n"
"s_lshl_b32 s60, s71, 7\n"
"s_add_u32 s66, s66, s60\n"
"s_mul_i32 s60, s71, 81920\n"
"s_mul_i32 s61, s72, 160\n"
"s_add_u32 s60, s60, s61\n"
"s_cmp_lt_u32 s71, 27\n"
"s_cselect_b32 s66, s66, 1073741824\n"
"s_cselect_b32 s60, s60, 1073741824\n"
"v_lshrrev_b32 v52, 3, v1\n"
"v_mul_u32_u24 v52, 3328, v52\n"
"v_add3_u32 v101, v52, v2, s66\n"
"v_add_u32 v52, 0, v0\n"
"v_mul_u32_u24 v53, 6554, v52\n"
"v_lshrrev_b32 v53, 16, v53\n"
"v_mul_u32_u24 v53, 1120, v53\n"
"v_lshl_add_u32 v52, v52, 4, v53\n"
"v_add_u32 v102, s60, v52\n"
"v_add_u32 v52, 256, v0\n"
"v_mul_u32_u24 v53, 6554, v52\n"
"v_lshrrev_b32 v53, 16, v53\n"
"v_mul_u32_u24 v53, 1120, v53\n"
"v_lshl_add_u32 v52, v52, 4, v53\n"
"v_add_u32 v103, s60, v52\n"
"v_add_u32 v52, 512, v0\n"
"v_mul_u32_u24 v53, 6554, v52\n"
"v_lshrrev_b32 v53, 16, v53\n"
"v_mul_u32_u24 v53, 1120, v53\n"
"v_lshl_add_u32 v52, v52, 4, v53\n"
"s_cmp_lt_u32 s40, 2\n"
"s_cselect_b32 s60, s60, 1073741824\n"
"v_add_u32 v104, s60, v52\n"
"s_lshl2_add_u32 s66, s46, s44\n"
"s_lshl_b32 s66, s66, 19\n"
"s_add_u32 s74, s66, 1048576\n"
"s_and_b32 s60, s73, 15\n"
"s_lshl4_add_u32 s60, s43, s60\n"
"s_lshl_b32 s60, s60, 14\n"
"s_add_u32 s60, s60, s74\n"
"s_lshl_b32 s61, s45, 7\n"
"s_add_u32 s83, s60, s61\n"
"s_addk_i32 s83, 10240\n"
"s_lshl_b32 s61, s45, 10\n"
"s_lshl2_add_u32 s77, s61, s60\n"
"s_add_u32 s76, s60, s61\n"
"s_addk_i32 s76, 8192\n"
"s_lshl2_add_u32 s66, s43, s44\n"
"s_mulk_i32 s66, 1280\n"
"s_addk_i32 s66, 28672\n"
"s_lshl_b32 s60, s45, 9\n"
"s_add_u32 s60, s60, s66\n"
"s_addk_i32 s66, 512\n"
"s_lshl_b32 s61, s73, 8\n"
"s_addk_i32 s61, 3328\n"
"v_mov_b32 v52, s60\n"
"v_mov_b32 v53, s66\n"
"v_cmp_lt_u32 vcc, 31, v1\n"
"v_mov_b32 v54, s61\n"
"s_mul_i32 s60, s40, 1536\n"
"s_add_u32 s60, s60, 155776\n"
"v_lshl_add_u32 v97, v1, 3, s60\n"
"v_cndmask_b32 v52, v52, v53, vcc\n"
"v_cmp_lt_u32 vcc, 47, v1\n"
"v_and_b32 v53, 12, v0\n"
"v_add_u32 v53, v53, v47\n"
"v_cndmask_b32 v52, v52, v54, vcc\n"
"v_add_u32 v96, v52, v2\n"
"v_lshl_add_u32 v98, v53, 2, s60\n"
"s_lshl_b32 s66, s45, 4\n"
"v_add_u32 v99, s66, v98\n"
"v_lshl_add_u32 v100, v48, 5, s60\n"
"s_lshl_b32 s66, s43, 31\n"
"v_mov_b32 v110, s66\n"
"s_waitcnt vmcnt(11)\n"
"buffer_load_dwordx4 v38, s[12:15], s79 offen offset:1024 lds\n"
"v_dot2_f32_bf16 v50, v200, s5, 0\n"
"v_dot2_f32_bf16 v50, v201, s5, v50\n"
"v_dot2_f32_bf16 v50, v202, s5, v50\n"
"v_dot2_f32_bf16 v50, v203, s5, v50\n"
"v_dot2_f32_bf16 v50, v204, s5, v50\n"
"v_dot2_f32_bf16 v50, v205, s5, v50\n"
"v_dot2_f32_bf16 v50, v206, s5, v50\n"
"v_dot2_f32_bf16 v50, v207, s5, v50\n"
"v_dot2_f32_bf16 v50, v208, s5, v50\n"
"v_dot2_f32_bf16 v50, v209, s5, v50\n"
"v_lshlrev_b32 v230, 16, v200\n"
"v_and_b32 v231, s4, v200\n"
"s_add_u32 m0, s70, 0\n"
"s_addk_i32 s79, 128\n"
"s_sub_u32 s75, s79, 1280\n"
"s_min_u32 s79, s79, s75\n"
"buffer_load_dwordx4 v39, s[12:15], s80 offen lds\n"
"v_lshlrev_b32 v232, 16, v201\n"
"v_and_b32 v233, s4, v201\n"
"v_add_f32_dpp v50, v50, v50 quad_perm:[1,0,3,2] row_mask:0xf bank_mask:0xf\n"
"v_lshlrev_b32 v234, 16, v202\n"
"v_and_b32 v235, s4, v202\n"
"v_add_f32_dpp v50, v50, v50 quad_perm:[2,3,0,1] row_mask:0xf bank_mask:0xf\n"
"v_lshlrev_b32 v236, 16, v203\n"
"v_and_b32 v237, s4, v203\n"
"v_add_f32_dpp v50, v50, v50 row_half_mirror row_mask:0xf bank_mask:0xf\n"
"v_lshlrev_b32 v238, 16, v204\n"
"v_and_b32 v239, s4, v204\n"
"s_addk_i32 s80, 256\n"
"s_sub_u32 s75, s80, 1280\n"
"s_min_u32 s80, s80, s75\n"
"s_add_u32 m0, s68, 24576\n"
"s_nop 0\n"
"buffer_load_dwordx4 v33, s[8:11], s79 offen offset:0 lds\n"
"v_add_f32_dpp v50, v50, v50 row_mirror row_mask:0xf bank_mask:0xf\n"
"v_lshlrev_b32 v240, 16, v205\n"
"v_and_b32 v241, s4, v205\n"
"v_mov_b32 v52, v50\n"
"v_lshlrev_b32 v242, 16, v206\n"
"v_and_b32 v243, s4, v206\n"
"v_permlane16_swap_b32 v52, v50\n"
"v_lshlrev_b32 v244, 16, v207\n"
"v_and_b32 v245, s4, v207\n"
"v_add_f32 v50, v50, v52\n"
"v_lshlrev_b32 v246, 16, v208\n"
"v_and_b32 v247, s4, v208\n"
"buffer_load_dwordx4 v34, s[8:11], s79 offen offset:1024 lds\n"
"v_mul_f32 v50, 0x3acccccd, v50\n"
"v_lshlrev_b32 v248, 16, v209\n"
"v_and_b32 v249, s4, v209\n"
"v_pk_add_f32 v[230:231], v[230:231], v[50:51] op_sel_hi:[1,0] neg_lo:[0,1] neg_hi:[0,1]\n"
"v_pk_add_f32 v[232:233], v[232:233], v[50:51] op_sel_hi:[1,0] neg_lo:[0,1] neg_hi:[0,1]\n"
"v_pk_add_f32 v[234:235], v[234:235], v[50:51] op_sel_hi:[1,0] neg_lo:[0,1] neg_hi:[0,1]\n"
"v_pk_add_f32 v[236:237], v[236:237], v[50:51] op_sel_hi:[1,0] neg_lo:[0,1] neg_hi:[0,1]\n"
"v_pk_add_f32 v[238:239], v[238:239], v[50:51] op_sel_hi:[1,0] neg_lo:[0,1] neg_hi:[0,1]\n"
"v_pk_add_f32 v[240:241], v[240:241], v[50:51] op_sel_hi:[1,0] neg_lo:[0,1] neg_hi:[0,1]\n"
"v_pk_add_f32 v[242:243], v[242:243], v[50:51] op_sel_hi:[1,0] neg_lo:[0,1] neg_hi:[0,1]\n"
"v_pk_add_f32 v[244:245], v[244:245], v[50:51] op_sel_hi:[1,0] neg_lo:[0,1] neg_hi:[0,1]\n"
"v_pk_add_f32 v[246:247], v[246:247], v[50:51] op_sel_hi:[1,0] neg_lo:[0,1] neg_hi:[0,1]\n"
"buffer_load_dwordx4 v35, s[8:11], s79 offen offset:2048 lds\n"
"v_pk_add_f32 v[248:249], v[248:249], v[50:51] op_sel_hi:[1,0] neg_lo:[0,1] neg_hi:[0,1]\n"
"v_pk_mul_f32 v[252:253], v[230:231], v[230:231]\n"
"v_pk_fma_f32 v[252:253], v[232:233], v[232:233], v[252:253]\n"
"v_pk_fma_f32 v[252:253], v[234:235], v[234:235], v[252:253]\n"
"v_pk_fma_f32 v[252:253], v[236:237], v[236:237], v[252:253]\n"
"v_pk_fma_f32 v[252:253], v[238:239], v[238:239], v[252:253]\n"
"v_pk_fma_f32 v[252:253], v[240:241], v[240:241], v[252:253]\n"
"v_pk_fma_f32 v[252:253], v[242:243], v[242:243], v[252:253]\n"
"v_pk_fma_f32 v[252:253], v[244:245], v[244:245], v[252:253]\n"
"v_pk_fma_f32 v[252:253], v[246:247], v[246:247], v[252:253]\n"
"v_pk_fma_f32 v[252:253], v[248:249], v[248:249], v[252:253]\n"
"v_add_f32 v46, v252, v253\n"
"buffer_load_dwordx4 v36, s[8:11], s79 offen offset:3072 lds\n"
"s_waitcnt vmcnt(11)\n"
"v_dot2_f32_bf16 v47, v220, s5, 0\n"
"v_dot2_f32_bf16 v48, v210, s5, 0\n"
"v_dot2_f32_bf16 v49, v200, v210, 0\n"
"v_dot2_f32_bf16 v47, v221, s5, v47\n"
"v_dot2_f32_bf16 v48, v211, s5, v48\n"
"v_dot2_f32_bf16 v49, v201, v211, v49\n"
"v_dot2_f32_bf16 v47, v222, s5, v47\n"
"v_dot2_f32_bf16 v48, v212, s5, v48\n"
"v_dot2_f32_bf16 v49, v202, v212, v49\n"
"v_dot2_f32_bf16 v47, v223, s5, v47\n"
"v_dot2_f32_bf16 v48, v213, s5, v48\n"
"s_add_u32 m0, s69, 24576\n"
"s_nop 0\n"
"buffer_load_dwordx4 v37, s[12:15], s79 offen offset:0 lds\n"
"v_dot2_f32_bf16 v49, v203, v213, v49\n"
"v_dot2_f32_bf16 v47, v224, s5, v47\n"
"v_dot2_f32_bf16 v48, v214, s5, v48\n"
"v_dot2_f32_bf16 v49, v204, v214, v49\n"
"v_dot2_f32_bf16 v47, v225, s5, v47\n"
"v_dot2_f32_bf16 v48, v215, s5, v48\n"
"v_dot2_f32_bf16 v49, v205, v215, v49\n"
"v_dot2_f32_bf16 v47, v226, s5, v47\n"
"v_dot2_f32_bf16 v48, v216, s5, v48\n"
"v_dot2_f32_bf16 v49, v206, v216, v49\n"
"v_dot2_f32_bf16 v47, v227, s5, v47\n"
"v_dot2_f32_bf16 v48, v217, s5, v48\n"
"buffer_load_dwordx4 v38, s[12:15], s79 offen offset:1024 lds\n"
"v_dot2_f32_bf16 v49, v207, v217, v49\n"
"v_dot2_f32_bf16 v47, v228, s5, v47\n"
"v_dot2_f32_bf16 v48, v218, s5, v48\n"
"v_dot2_f32_bf16 v49, v208, v218, v49\n"
"v_dot2_f32_bf16 v47, v229, s5, v47\n"
"v_dot2_f32_bf16 v48, v219, s5, v48\n"
"v_dot2_f32_bf16 v49, v209, v219, v49\n"
"v_add_f32_dpp v46, v46, v46 quad_perm:[1,0,3,2] row_mask:0xf bank_mask:0xf\n"
"v_add_f32_dpp v47, v47, v47 quad_perm:[1,0,3,2] row_mask:0xf bank_mask:0xf\n"
"v_add_f32_dpp v48, v48, v48 quad_perm:[1,0,3,2] row_mask:0xf bank_mask:0xf\n"
"v_add_f32_dpp v49, v49, v49 quad_perm:[1,0,3,2] row_mask:0xf bank_mask:0xf\n"
"v_add_f32_dpp v46, v46, v46 quad_perm:[2,3,0,1] row_mask:0xf bank_mask:0xf\n"
"s_addk_i32 s79, 128\n"
"s_sub_u32 s75, s79, 1280\n"
"s_min_u32 s79, s79, s75\n"
"s_add_u32 m0, s68, 49152\n"
"s_nop 0\n"
"buffer_load_dwordx4 v33, s[8:11], s79 offen offset:0 lds\n"
"v_add_f32_dpp v47, v47, v47 quad_perm:[2,3,0,1] row_mask:0xf bank_mask:0xf\n"
"v_add_f32_dpp v48, v48, v48 quad_perm:[2,3,0,1] row_mask:0xf bank_mask:0xf\n"
"v_add_f32_dpp v49, v49, v49 quad_perm:[2,3,0,1] row_mask:0xf bank_mask:0xf\n"
"v_add_f32_dpp v46, v46, v46 row_half_mirror row_mask:0xf bank_mask:0xf\n"
"v_add_f32_dpp v47, v47, v47 row_half_mirror row_mask:0xf bank_mask:0xf\n"
"v_add_f32_dpp v48, v48, v48 row_half_mirror row_mask:0xf bank_mask:0xf\n"
"v_add_f32_dpp v49, v49, v49 row_half_mirror row_mask:0xf bank_mask:0xf\n"
"v_add_f32_dpp v46, v46, v46 row_mirror row_mask:0xf bank_mask:0xf\n"
"v_add_f32_dpp v47, v47, v47 row_mirror row_mask:0xf bank_mask:0xf\n"
"v_add_f32_dpp v48, v48, v48 row_mirror row_mask:0xf bank_mask:0xf\n"
"v_add_f32_dpp v49, v49, v49 row_mirror row_mask:0xf bank_mask:0xf\n"
"v_mov_b32 v52, v46\n"
"buffer_load_dwordx4 v34, s[8:11], s79 offen offset:1024 lds\n"
"v_mov_b32 v53, v47\n"
"v_mov_b32 v54, v48\n"
"v_mov_b32 v55, v49\n"
"s_waitcnt lgkmcnt(0)\n"
"v_permlane16_swap_b32 v52, v46\n"
"v_permlane16_swap_b32 v53, v47\n"
"v_permlane16_swap_b32 v54, v48\n"
"v_permlane16_swap_b32 v55, v49\n"
"v_pk_add_f32 v[46:47], v[46:47], v[52:53]\n"
"s_add_u32 s3, s3, 1\n"
"v_pk_add_f32 v[48:49], v[48:49], v[54:55]\n"
"s_mul_i32 s47, s3, 0x9E3779B1\n"
"buffer_load_dwordx4 v35, s[8:11], s79 offen offset:2048 lds\n"
"v_mul_f32 v46, 0x3acccccd, v46\n"
"s_lshl_b32 s60, s64, 6\n"
"v_cvt_pk_bf16_f32 v46, v50, v46\n"
"s_addk_i32 s60, 4096\n"
"v_lshlrev_b32 v50, 16, v46\n"
"s_lshl2_add_u32 s61, s64, s40\n"
"v_and_b32 v46, s4, v46\n"
"s_lshl3_add_u32 s61, s61, 20480\n"
"v_add_f32 v46, 0x3727c5ac, v46\n"
"s_mul_i32 s66, s43, 80\n"
"v_cvt_pk_bf16_f32 v46, v46, v46\n"
"s_add_u32 s66, s66, s65\n"
"buffer_load_dwordx4 v36, s[8:11], s79 offen offset:3072 lds\n"
"v_and_b32 v46, s4, v46\n"
"s_lshl_b32 s66, s66, 6\n"
"v_sqrt_f32 v46, v46\n"
"s_addk_i32 s66, 28672\n"
"v_fma_f32 v49, -v50, v48, v49\n"
"s_cmp_lt_u32 s41, 20\n"
"s_cselect_b32 s66, s66, 1073741824\n"
"v_cvt_pk_bf16_f32 v230, v50, v46\n"
"v_and_b32 v46, s4, v230\n"
"v_rcp_f32 v46, v46\n"
"v_mov_b32 v231, s47\n"
"v_mul_f32 v49, v49, v46\n"
"v_mov_b32 v52, v49\n"
"s_add_u32 m0, s69, 49152\n"
"s_nop 0\n"
"buffer_load_dwordx4 v37, s[12:15], s79 offen offset:0 lds\n"
"v_mov_b32 v233, s47\n"
"v_mov_b32 v235, s47\n"
"v_permlane32_swap_b32 v49, v52\n"
"v_cvt_pk_bf16_f32 v232, v49, v52\n"
"v_mov_b32 v234, v47\n"
"v_lshlrev_b32 v44, 3, v44\n"
"buffer_load_dwordx4 v38, s[12:15], s79 offen offset:1024 lds\n"
"s_add_u32 m0, s70, 4096\n"
"s_addk_i32 s79, 128\n"
"s_sub_u32 s75, s79, 1280\n"
"s_min_u32 s79, s79, s75\n"
"buffer_load_dwordx4 v39, s[12:15], s80 offen lds\n"
"s_mov_b32 exec_lo, 1\n"
"s_mov_b32 exec_hi, 1\n"
"buffer_store_dwordx2 v[230:231], v44, s[20:23], s60 offen sc1\n"
"buffer_store_dwordx2 v[234:235], v44, s[20:23], s66 offen sc1\n"
"s_mov_b32 exec_hi, 0\n"
"buffer_store_dwordx2 v[232:233], off, s[20:23], s61 sc1\n"
"s_mov_b64 exec, -1\n"
"s_addk_i32 s80, 256\n"
"s_sub_u32 s75, s80, 1280\n"
"s_min_u32 s80, s80, s75\n"
"s_add_u32 m0, s68, 73728\n"
"s_nop 0\n"
"buffer_load_dwordx4 v33, s[8:11], s79 offen offset:0 lds\n"
"buffer_load_dwordx4 v34, s[8:11], s79 offen offset:1024 lds\n"
"buffer_load_dwordx4 v35, s[8:11], s79 offen offset:2048 lds\n"
"buffer_load_dwordx4 v36, s[8:11], s79 offen offset:3072 lds\n"
"s_add_u32 m0, s69, 73728\n"
"s_nop 0\n"
"buffer_load_dwordx4 v37, s[12:15], s79 offen offset:0 lds\n"
"buffer_load_dwordx4 v38, s[12:15], s79 offen offset:1024 lds\n"
"s_waitcnt vmcnt(22)\n"
"s_barrier\n"
"s_cmp_eq_u32 s55, 0\n"
"s_cselect_b64 exec, -1, 0\n"
"ds_read_b128 v[20:23], v105\n"
"ds_read_b128 v[24:27], v106\n"
"s_mov_b64 exec, -1\n"
"s_cmp_eq_u32 s56, 0\n"
"s_cselect_b64 exec, -1, 0\n"
"ds_read_b64 v[28:29], v107\n"
"s_mov_b64 exec, -1\n"
"ds_read_b128 a[0:3], v40 offset:0\n"
"ds_read_b128 a[16:19], v42 offset:0\n"
"ds_read_b128 a[4:7], v41 offset:0\n"
"ds_read_b128 a[20:23], v43 offset:0\n"
"ds_read_b128 a[8:11], v40 offset:2048\n"
"ds_read_b128 a[12:15], v41 offset:2048\n"
"ds_read_b128 a[24:27], v42 offset:2048\n"
"ds_read_b128 a[28:31], v43 offset:2048\n"
"s_waitcnt lgkmcnt(4)\n"
"v_mfma_f32_16x16x32_bf16 v[56:59], a[0:3], a[16:19], 0\n"
"ds_read_b128 a[32:35], v42 offset:4096\n"
"s_addk_i32 s79, 128\n"
"s_sub_u32 s75, s79, 1280\n"
"s_min_u32 s79, s79, s75\n"
"s_add_u32 m0, s68, 98304\n"
"s_nop 0\n"
"buffer_load_dwordx4 v33, s[8:11], s79 offen offset:0 lds\n"
"v_mfma_f32_16x16x32_bf16 v[56:59], a[4:7], a[20:23], v[56:59]\n"
"ds_read_b128 a[36:39], v43 offset:4096\n"
"s_waitcnt lgkmcnt(4)\n"
"v_mfma_f32_16x16x32_bf16 v[76:79], a[8:11], a[16:19], 0\n"
"ds_read_b128 a[40:43], v42 offset:6144\n"
"v_mfma_f32_16x16x32_bf16 v[76:79], a[12:15], a[20:23], v[76:79]\n"
"ds_read_b128 a[44:47], v43 offset:6144\n"
"buffer_load_dwordx4 v34, s[8:11], s79 offen offset:1024 lds\n"
"s_waitcnt lgkmcnt(4)\n"
"v_mfma_f32_16x16x32_bf16 v[60:63], a[0:3], a[24:27], 0\n"
"ds_read_b128 a[48:51], v113 offset:32768\n"
"v_mfma_f32_16x16x32_bf16 v[60:63], a[4:7], a[28:31], v[60:63]\n"
"ds_read_b128 a[52:55], v114 offset:32768\n"
"v_mfma_f32_16x16x32_bf16 v[80:83], a[8:11], a[24:27], 0\n"
"buffer_load_dwordx4 v35, s[8:11], s79 offen offset:2048 lds\n"
"v_mfma_f32_16x16x32_bf16 v[80:83], a[12:15], a[28:31], v[80:83]\n"
"s_waitcnt lgkmcnt(4)\n"
"v_mfma_f32_16x16x32_bf16 v[64:67], a[0:3], a[32:35], 0\n"
"buffer_load_dwordx4 v36, s[8:11], s79 offen offset:3072 lds\n"
"v_mfma_f32_16x16x32_bf16 v[64:67], a[4:7], a[36:39], v[64:67]\n"
"v_mfma_f32_16x16x32_bf16 v[84:87], a[8:11], a[32:35], 0\n"
"v_mfma_f32_16x16x32_bf16 v[84:87], a[12:15], a[36:39], v[84:87]\n"
"s_add_u32 m0, s69, 98304\n"
"s_nop 0\n"
"buffer_load_dwordx4 v37, s[12:15], s79 offen offset:0 lds\n"
"s_waitcnt vmcnt(21) lgkmcnt(0)\n"
"s_barrier\n"
"v_mfma_f32_16x16x32_bf16 v[68:71], a[0:3], a[40:43], 0\n"
"ds_read_b128 a[56:59], v40 offset:24576\n"
"s_cmp_eq_u32 s55, 1\n"
"s_cselect_b64 exec, -1, 0\n"
"ds_read_b128 v[20:23], v105\n"
"ds_read_b128 v[24:27], v106\n"
"s_mov_b64 exec, -1\n"
"v_mfma_f32_16x16x32_bf16 v[68:71], a[4:7], a[44:47], v[68:71]\n"
"ds_read_b128 a[72:75], v42 offset:24576\n"
"buffer_load_dwordx4 v38, s[12:15], s79 offen offset:1024 lds\n"
"v_mfma_f32_16x16x32_bf16 v[88:91], a[8:11], a[40:43], 0\n"
"ds_read_b128 a[60:63], v41 offset:24576\n"
"s_cmp_eq_u32 s56, 1\n"
"s_cselect_b64 exec, -1, 0\n"
"ds_read_b64 v[28:29], v107\n"
"s_mov_b64 exec, -1\n"
"v_mfma_f32_16x16x32_bf16 v[88:91], a[12:15], a[44:47], v[88:91]\n"
"ds_read_b128 a[76:79], v43 offset:24576\n"
"v_mfma_f32_16x16x32_bf16 v[72:75], a[0:3], a[48:51], 0\n"
"ds_read_b128 a[64:67], v40 offset:26624\n"
"s_add_u32 m0, s70, 8192\n"
"s_addk_i32 s79, 128\n"
"s_sub_u32 s75, s79, 1280\n"
"s_min_u32 s79, s79, s75\n"
"buffer_load_dwordx4 v39, s[12:15], s80 offen lds\n"
"v_mfma_f32_16x16x32_bf16 v[72:75], a[4:7], a[52:55], v[72:75]\n"
"ds_read_b128 a[68:71], v41 offset:26624\n"
"v_mfma_f32_16x16x32_bf16 v[92:95], a[8:11], a[48:51], 0\n"
"ds_read_b128 a[80:83], v42 offset:26624\n"
"v_mfma_f32_16x16x32_bf16 v[92:95], a[12:15], a[52:55], v[92:95]\n"
"ds_read_b128 a[84:87], v43 offset:26624\n"
"s_addk_i32 s80, 256\n"
"s_sub_u32 s75, s80, 1280\n"
"s_min_u32 s80, s80, s75\n"
"s_add_u32 m0, s68, 0\n"
"s_nop 0\n"
"buffer_load_dwordx4 v33, s[8:11], s79 offen offset:0 lds\n"
"s_waitcnt lgkmcnt(4)\n"
"v_mfma_f32_16x16x32_bf16 v[56:59], a[56:59], a[72:75], v[56:59]\n"
"ds_read_b128 a[88:91], v42 offset:28672\n"
"v_mfma_f32_16x16x32_bf16 v[56:59], a[60:63], a[76:79], v[56:59]\n"
"ds_read_b128 a[92:95], v43 offset:28672\n"
"buffer_load_dwordx4 v34, s[8:11], s79 offen offset:1024 lds\n"
"s_waitcnt lgkmcnt(4)\n"
"v_mfma_f32_16x16x32_bf16 v[76:79], a[64:67], a[72:75], v[76:79]\n"
"ds_read_b128 a[96:99], v42 offset:30720\n"
"v_mfma_f32_16x16x32_bf16 v[76:79], a[68:71], a[76:79], v[76:79]\n"
"ds_read_b128 a[100:103], v43 offset:30720\n"
"s_waitcnt lgkmcnt(4)\n"
"v_mfma_f32_16x16x32_bf16 v[60:63], a[56:59], a[80:83], v[60:63]\n"
"ds_read_b128 a[104:107], v113 offset:34816\n"
"buffer_load_dwordx4 v35, s[8:11], s79 offen offset:2048 lds\n"
"v_mfma_f32_16x16x32_bf16 v[60:63], a[60:63], a[84:87], v[60:63]\n"
"ds_read_b128 a[108:111], v114 offset:34816\n"
"v_mfma_f32_16x16x32_bf16 v[80:83], a[64:67], a[80:83], v[80:83]\n"
"v_mfma_f32_16x16x32_bf16 v[80:83], a[68:71], a[84:87], v[80:83]\n"
"buffer_load_dwordx4 v36, s[8:11], s79 offen offset:3072 lds\n"
"s_waitcnt lgkmcnt(4)\n"
"v_mfma_f32_16x16x32_bf16 v[64:67], a[56:59], a[88:91], v[64:67]\n"
"v_mfma_f32_16x16x32_bf16 v[64:67], a[60:63], a[92:95], v[64:67]\n"
"s_add_u32 m0, s69, 0\n"
"s_nop 0\n"
"buffer_load_dwordx4 v37, s[12:15], s79 offen offset:0 lds\n"
"v_mfma_f32_16x16x32_bf16 v[84:87], a[64:67], a[88:91], v[84:87]\n"
"v_mfma_f32_16x16x32_bf16 v[84:87], a[68:71], a[92:95], v[84:87]\n"
"s_waitcnt vmcnt(21) lgkmcnt(0)\n"
"s_barrier\n"
"v_mfma_f32_16x16x32_bf16 v[68:71], a[56:59], a[96:99], v[68:71]\n"
"ds_read_b128 a[0:3], v40 offset:49152\n"
"buffer_load_dwordx4 v38, s[12:15], s79 offen offset:1024 lds\n"
"v_mfma_f32_16x16x32_bf16 v[68:71], a[60:63], a[100:103], v[68:71]\n"
"ds_read_b128 a[16:19], v42 offset:49152\n"
"s_cmp_eq_u32 s55, 2\n"
"s_cselect_b64 exec, -1, 0\n"
"ds_read_b128 v[20:23], v105\n"
"ds_read_b128 v[24:27], v106\n"
"s_mov_b64 exec, -1\n"
"v_mfma_f32_16x16x32_bf16 v[88:91], a[64:67], a[96:99], v[88:91]\n"
"ds_read_b128 a[4:7], v41 offset:49152\n"
"s_addk_i32 s79, 128\n"
"s_sub_u32 s75, s79, 1280\n"
"s_min_u32 s79, s79, s75\n"
"s_add_u32 m0, s68, 24576\n"
"s_nop 0\n"
"buffer_load_dwordx4 v33, s[8:11], s79 offen offset:0 lds\n"
"v_mfma_f32_16x16x32_bf16 v[88:91], a[68:71], a[100:103], v[88:91]\n"
"ds_read_b128 a[20:23], v43 offset:49152\n"
"s_cmp_eq_u32 s56, 2\n"
"s_cselect_b64 exec, -1, 0\n"
"ds_read_b64 v[28:29], v107\n"
"s_mov_b64 exec, -1\n"
"v_mfma_f32_16x16x32_bf16 v[72:75], a[56:59], a[104:107], v[72:75]\n"
"ds_read_b128 a[8:11], v40 offset:51200\n"
"v_mfma_f32_16x16x32_bf16 v[72:75], a[60:63], a[108:111], v[72:75]\n"
"ds_read_b128 a[12:15], v41 offset:51200\n"
"buffer_load_dwordx4 v34, s[8:11], s79 offen offset:1024 lds\n"
"v_mfma_f32_16x16x32_bf16 v[92:95], a[64:67], a[104:107], v[92:95]\n"
"ds_read_b128 a[24:27], v42 offset:51200\n"
"v_mfma_f32_16x16x32_bf16 v[92:95], a[68:71], a[108:111], v[92:95]\n"
"ds_read_b128 a[28:31], v43 offset:51200\n"
"s_waitcnt lgkmcnt(4)\n"
"v_mfma_f32_16x16x32_bf16 v[56:59], a[0:3], a[16:19], v[56:59]\n"
"ds_read_b128 a[32:35], v42 offset:53248\n"
"buffer_load_dwordx4 v35, s[8:11], s79 offen offset:2048 lds\n"
"v_mfma_f32_16x16x32_bf16 v[56:59], a[4:7], a[20:23], v[56:59]\n"
"ds_read_b128 a[36:39], v43 offset:53248\n"
"s_waitcnt lgkmcnt(4)\n"
"v_mfma_f32_16x16x32_bf16 v[76:79], a[8:11], a[16:19], v[76:79]\n"
"ds_read_b128 a[40:43], v42 offset:55296\n"
"buffer_load_dwordx4 v36, s[8:11], s79 offen offset:3072 lds\n"
"v_mfma_f32_16x16x32_bf16 v[76:79], a[12:15], a[20:23], v[76:79]\n"
"ds_read_b128 a[44:47], v43 offset:55296\n"
"s_waitcnt lgkmcnt(4)\n"
"v_mfma_f32_16x16x32_bf16 v[60:63], a[0:3], a[24:27], v[60:63]\n"
"ds_read_b128 a[48:51], v113 offset:36864\n"
"v_mfma_f32_16x16x32_bf16 v[60:63], a[4:7], a[28:31], v[60:63]\n"
"ds_read_b128 a[52:55], v114 offset:36864\n"
"s_add_u32 m0, s69, 24576\n"
"s_nop 0\n"
"buffer_load_dwordx4 v37, s[12:15], s79 offen offset:0 lds\n"
"v_mfma_f32_16x16x32_bf16 v[80:83], a[8:11], a[24:27], v[80:83]\n"
"v_mfma_f32_16x16x32_bf16 v[80:83], a[12:15], a[28:31], v[80:83]\n"
"buffer_load_dwordx4 v38, s[12:15], s79 offen offset:1024 lds\n"
"s_waitcnt lgkmcnt(4)\n"
"v_mfma_f32_16x16x32_bf16 v[64:67], a[0:3], a[32:35], v[64:67]\n"
"v_mfma_f32_16x16x32_bf16 v[64:67], a[4:7], a[36:39], v[64:67]\n"
"v_mfma_f32_16x16x32_bf16 v[84:87], a[8:11], a[32:35], v[84:87]\n"
"s_add_u32 m0, s70, 0\n"
"s_addk_i32 s79, 128\n"
"s_sub_u32 s75, s79, 1280\n"
"s_min_u32 s79, s79, s75\n"
"buffer_load_dwordx4 v39, s[12:15], s80 offen lds\n"
"v_mfma_f32_16x16x32_bf16 v[84:87], a[12:15], a[36:39], v[84:87]\n"
"s_waitcnt vmcnt(20) lgkmcnt(0)\n"
"s_barrier\n"
"v_mfma_f32_16x16x32_bf16 v[68:71], a[0:3], a[40:43], v[68:71]\n"
"ds_read_b128 a[56:59], v111 offset:0\n"
"s_cmp_eq_u32 s55, 3\n"
"s_cselect_b64 exec, -1, 0\n"
"ds_read_b128 v[20:23], v105\n"
"ds_read_b128 v[24:27], v106\n"
"s_mov_b64 exec, -1\n"
"v_mfma_f32_16x16x32_bf16 v[68:71], a[4:7], a[44:47], v[68:71]\n"
"ds_read_b128 a[72:75], v113 offset:0\n"
"s_addk_i32 s80, 256\n"
"s_sub_u32 s75, s80, 1280\n"
"s_min_u32 s80, s80, s75\n"
"s_add_u32 m0, s68, 49152\n"
"s_nop 0\n"
"buffer_load_dwordx4 v33, s[8:11], s79 offen offset:0 lds\n"
"v_mfma_f32_16x16x32_bf16 v[88:91], a[8:11], a[40:43], v[88:91]\n"
"ds_read_b128 a[60:63], v112 offset:0\n"
"s_cmp_eq_u32 s56, 3\n"
"s_cselect_b64 exec, -1, 0\n"
"ds_read_b64 v[28:29], v107\n"
"s_mov_b64 exec, -1\n"
"v_mfma_f32_16x16x32_bf16 v[88:91], a[12:15], a[44:47], v[88:91]\n"
"ds_read_b128 a[76:79], v114 offset:0\n"
"buffer_load_dwordx4 v34, s[8:11], s79 offen offset:1024 lds\n"
"v_mfma_f32_16x16x32_bf16 v[72:75], a[0:3], a[48:51], v[72:75]\n"
"ds_read_b128 a[64:67], v111 offset:2048\n"
"v_mfma_f32_16x16x32_bf16 v[72:75], a[4:7], a[52:55], v[72:75]\n"
"ds_read_b128 a[68:71], v112 offset:2048\n"
"v_mfma_f32_16x16x32_bf16 v[92:95], a[8:11], a[48:51], v[92:95]\n"
"ds_read_b128 a[80:83], v113 offset:2048\n"
"buffer_load_dwordx4 v35, s[8:11], s79 offen offset:2048 lds\n"
"v_mfma_f32_16x16x32_bf16 v[92:95], a[12:15], a[52:55], v[92:95]\n"
"ds_read_b128 a[84:87], v114 offset:2048\n"
"s_waitcnt lgkmcnt(4)\n"
"v_mfma_f32_16x16x32_bf16 v[56:59], a[56:59], a[72:75], v[56:59]\n"
"ds_read_b128 a[88:91], v113 offset:4096\n"
"buffer_load_dwordx4 v36, s[8:11], s79 offen offset:3072 lds\n"
"v_mfma_f32_16x16x32_bf16 v[56:59], a[60:63], a[76:79], v[56:59]\n"
"ds_read_b128 a[92:95], v114 offset:4096\n"
"s_waitcnt lgkmcnt(4)\n"
"v_mfma_f32_16x16x32_bf16 v[76:79], a[64:67], a[72:75], v[76:79]\n"
"ds_read_b128 a[96:99], v113 offset:6144\n"
"v_mfma_f32_16x16x32_bf16 v[76:79], a[68:71], a[76:79], v[76:79]\n"
"ds_read_b128 a[100:103], v114 offset:6144\n"
"s_add_u32 m0, s69, 49152\n"
"s_nop 0\n"
"buffer_load_dwordx4 v37, s[12:15], s79 offen offset:0 lds\n"
"s_waitcnt lgkmcnt(4)\n"
"v_mfma_f32_16x16x32_bf16 v[60:63], a[56:59], a[80:83], v[60:63]\n"
"ds_read_b128 a[104:107], v113 offset:38912\n"
"v_mfma_f32_16x16x32_bf16 v[60:63], a[60:63], a[84:87], v[60:63]\n"
"ds_read_b128 a[108:111], v114 offset:38912\n"
"v_mfma_f32_16x16x32_bf16 v[80:83], a[64:67], a[80:83], v[80:83]\n"
"buffer_load_dwordx4 v38, s[12:15], s79 offen offset:1024 lds\n"
"v_mfma_f32_16x16x32_bf16 v[80:83], a[68:71], a[84:87], v[80:83]\n"
"s_waitcnt lgkmcnt(4)\n"
"v_mfma_f32_16x16x32_bf16 v[64:67], a[56:59], a[88:91], v[64:67]\n"
"s_addk_i32 s79, 128\n"
"s_sub_u32 s75, s79, 1280\n"
"s_min_u32 s79, s79, s75\n"
"v_mfma_f32_16x16x32_bf16 v[64:67], a[60:63], a[92:95], v[64:67]\n"
"v_mfma_f32_16x16x32_bf16 v[84:87], a[64:67], a[88:91], v[84:87]\n"
"v_mfma_f32_16x16x32_bf16 v[84:87], a[68:71], a[92:95], v[84:87]\n"
"s_waitcnt vmcnt(19) lgkmcnt(0)\n"
"s_barrier\n"
"v_mfma_f32_16x16x32_bf16 v[68:71], a[56:59], a[96:99], v[68:71]\n"
"ds_read_b128 a[0:3], v111 offset:24576\n"
"s_add_u32 m0, s68, 73728\n"
"s_nop 0\n"
"buffer_load_dwordx4 v33, s[8:11], s79 offen offset:0 lds\n"
"v_mfma_f32_16x16x32_bf16 v[68:71], a[60:63], a[100:103], v[68:71]\n"
"ds_read_b128 a[16:19], v113 offset:24576\n"
"s_cmp_eq_u32 s55, 4\n"
"s_cselect_b64 exec, -1, 0\n"
"ds_read_b128 v[20:23], v105\n"
"ds_read_b128 v[24:27], v106\n"
"s_mov_b64 exec, -1\n"
"v_mfma_f32_16x16x32_bf16 v[88:91], a[64:67], a[96:99], v[88:91]\n"
"ds_read_b128 a[4:7], v112 offset:24576\n"
"buffer_load_dwordx4 v34, s[8:11], s79 offen offset:1024 lds\n"
"v_mfma_f32_16x16x32_bf16 v[88:91], a[68:71], a[100:103], v[88:91]\n"
"ds_read_b128 a[20:23], v114 offset:24576\n"
"s_cmp_eq_u32 s56, 4\n"
"s_cselect_b64 exec, -1, 0\n"
"ds_read_b64 v[28:29], v107\n"
"s_mov_b64 exec, -1\n"
"v_mfma_f32_16x16x32_bf16 v[72:75], a[56:59], a[104:107], v[72:75]\n"
"ds_read_b128 a[8:11], v111 offset:26624\n"
"v_mfma_f32_16x16x32_bf16 v[72:75], a[60:63], a[108:111], v[72:75]\n"
"ds_read_b128 a[12:15], v112 offset:26624\n"
"buffer_load_dwordx4 v35, s[8:11], s79 offen offset:2048 lds\n"
"v_mfma_f32_16x16x32_bf16 v[92:95], a[64:67], a[104:107], v[92:95]\n"
"ds_read_b128 a[24:27], v113 offset:26624\n"
"v_mfma_f32_16x16x32_bf16 v[92:95], a[68:71], a[108:111], v[92:95]\n"
"ds_read_b128 a[28:31], v114 offset:26624\n"
"buffer_load_dwordx4 v36, s[8:11], s79 offen offset:3072 lds\n"
"s_waitcnt lgkmcnt(4)\n"
"v_mfma_f32_16x16x32_bf16 v[56:59], a[0:3], a[16:19], v[56:59]\n"
"ds_read_b128 a[32:35], v113 offset:28672\n"
"v_mfma_f32_16x16x32_bf16 v[56:59], a[4:7], a[20:23], v[56:59]\n"
"ds_read_b128 a[36:39], v114 offset:28672\n"
"s_waitcnt lgkmcnt(4)\n"
"v_mfma_f32_16x16x32_bf16 v[76:79], a[8:11], a[16:19], v[76:79]\n"
"ds_read_b128 a[40:43], v113 offset:30720\n"
"s_add_u32 m0, s69, 73728\n"
"s_nop 0\n"
"buffer_load_dwordx4 v37, s[12:15], s79 offen offset:0 lds\n"
"v_mfma_f32_16x16x32_bf16 v[76:79], a[12:15], a[20:23], v[76:79]\n"
"ds_read_b128 a[44:47], v114 offset:30720\n"
"s_waitcnt lgkmcnt(4)\n"
"v_mfma_f32_16x16x32_bf16 v[60:63], a[0:3], a[24:27], v[60:63]\n"
"ds_read_b128 a[48:51], v113 offset:40960\n"
"buffer_load_dwordx4 v38, s[12:15], s79 offen offset:1024 lds\n"
"v_mfma_f32_16x16x32_bf16 v[60:63], a[4:7], a[28:31], v[60:63]\n"
"ds_read_b128 a[52:55], v114 offset:40960\n"
"v_mfma_f32_16x16x32_bf16 v[80:83], a[8:11], a[24:27], v[80:83]\n"
"v_mfma_f32_16x16x32_bf16 v[80:83], a[12:15], a[28:31], v[80:83]\n"
"s_add_u32 m0, s70, 4096\n"
"s_addk_i32 s79, 128\n"
"s_sub_u32 s75, s79, 1280\n"
"s_min_u32 s79, s79, s75\n"
"buffer_load_dwordx4 v39, s[12:15], s80 offen lds\n"
"s_waitcnt lgkmcnt(4)\n"
"v_mfma_f32_16x16x32_bf16 v[64:67], a[0:3], a[32:35], v[64:67]\n"
"v_mfma_f32_16x16x32_bf16 v[64:67], a[4:7], a[36:39], v[64:67]\n"
"v_mfma_f32_16x16x32_bf16 v[84:87], a[8:11], a[32:35], v[84:87]\n"
"v_mfma_f32_16x16x32_bf16 v[84:87], a[12:15], a[36:39], v[84:87]\n"
"s_waitcnt vmcnt(20) lgkmcnt(0)\n"
"s_barrier\n"
"v_mfma_f32_16x16x32_bf16 v[68:71], a[0:3], a[40:43], v[68:71]\n"
"ds_read_b128 a[56:59], v40 offset:0\n"
"s_add_u32 m0, s68, 98304\n"
"s_nop 0\n"
"buffer_load_dwordx4 v33, s[8:11], s79 offen offset:0 lds\n"
"v_mfma_f32_16x16x32_bf16 v[68:71], a[4:7], a[44:47], v[68:71]\n"
"ds_read_b128 a[72:75], v42 offset:0\n"
"s_cmp_eq_u32 s55, 5\n"
"s_cselect_b64 exec, -1, 0\n"
"ds_read_b128 v[20:23], v105\n"
"ds_read_b128 v[24:27], v106\n"
"s_mov_b64 exec, -1\n"
"v_mfma_f32_16x16x32_bf16 v[88:91], a[8:11], a[40:43], v[88:91]\n"
"ds_read_b128 a[60:63], v41 offset:0\n"
"s_cmp_eq_u32 s56, 5\n"
"s_cselect_b64 exec, -1, 0\n"
"ds_read_b64 v[28:29], v107\n"
"s_mov_b64 exec, -1\n"
"v_mfma_f32_16x16x32_bf16 v[88:91], a[12:15], a[44:47], v[88:91]\n"
"ds_read_b128 a[76:79], v43 offset:0\n"
"buffer_load_dwordx4 v34, s[8:11], s79 offen offset:1024 lds\n"
"v_mfma_f32_16x16x32_bf16 v[72:75], a[0:3], a[48:51], v[72:75]\n"
"ds_read_b128 a[64:67], v40 offset:2048\n"
"v_mfma_f32_16x16x32_bf16 v[72:75], a[4:7], a[52:55], v[72:75]\n"
"ds_read_b128 a[68:71], v41 offset:2048\n"
"buffer_load_dwordx4 v35, s[8:11], s79 offen offset:2048 lds\n"
"v_mfma_f32_16x16x32_bf16 v[92:95], a[8:11], a[48:51], v[92:95]\n"
"ds_read_b128 a[80:83], v42 offset:2048\n"
"v_mfma_f32_16x16x32_bf16 v[92:95], a[12:15], a[52:55], v[92:95]\n"
"ds_read_b128 a[84:87], v43 offset:2048\n"
"s_waitcnt lgkmcnt(4)\n"
"v_mfma_f32_16x16x32_bf16 v[56:59], a[56:59], a[72:75], v[56:59]\n"
"ds_read_b128 a[88:91], v42 offset:4096\n"
"buffer_load_dwordx4 v36, s[8:11], s79 offen offset:3072 lds\n"
"v_mfma_f32_16x16x32_bf16 v[56:59], a[60:63], a[76:79], v[56:59]\n"
"ds_read_b128 a[92:95], v43 offset:4096\n"
"s_waitcnt lgkmcnt(4)\n"
"v_mfma_f32_16x16x32_bf16 v[76:79], a[64:67], a[72:75], v[76:79]\n"
"ds_read_b128 a[96:99], v42 offset:6144\n"
"v_mfma_f32_16x16x32_bf16 v[76:79], a[68:71], a[76:79], v[76:79]\n"
"ds_read_b128 a[100:103], v43 offset:6144\n"
"s_add_u32 m0, s69, 98304\n"
"s_nop 0\n"
"buffer_load_dwordx4 v37, s[12:15], s79 offen offset:0 lds\n"
"s_waitcnt lgkmcnt(4)\n"
"v_mfma_f32_16x16x32_bf16 v[60:63], a[56:59], a[80:83], v[60:63]\n"
"ds_read_b128 a[104:107], v113 offset:43008\n"
"v_mfma_f32_16x16x32_bf16 v[60:63], a[60:63], a[84:87], v[60:63]\n"
"ds_read_b128 a[108:111], v114 offset:43008\n"
"buffer_load_dwordx4 v38, s[12:15], s79 offen offset:1024 lds\n"
"v_mfma_f32_16x16x32_bf16 v[80:83], a[64:67], a[80:83], v[80:83]\n"
"v_mfma_f32_16x16x32_bf16 v[80:83], a[68:71], a[84:87], v[80:83]\n"
"s_waitcnt lgkmcnt(4)\n"
"v_mfma_f32_16x16x32_bf16 v[64:67], a[56:59], a[88:91], v[64:67]\n"
"buffer_load_dwordx4 v[160:163], v96, s[20:23], 0 offen sc1\n"
"v_mfma_f32_16x16x32_bf16 v[64:67], a[60:63], a[92:95], v[64:67]\n"
"v_mfma_f32_16x16x32_bf16 v[84:87], a[64:67], a[88:91], v[84:87]\n"
"s_lshl_b32 s64, s40, 11\n"
"s_addk_i32 s64, 20480\n"
"buffer_load_dwordx4 v[200:203], v2, s[20:23], s64 offen offset:0 sc1\n"
"v_mfma_f32_16x16x32_bf16 v[84:87], a[68:71], a[92:95], v[84:87]\n"
"s_waitcnt vmcnt(21) lgkmcnt(0)\n"
"s_barrier\n"
"v_mfma_f32_16x16x32_bf16 v[68:71], a[56:59], a[96:99], v[68:71]\n"
"ds_read_b128 a[0:3], v40 offset:24576\n"
"s_cmp_eq_u32 s55, 6\n"
"s_cselect_b64 exec, -1, 0\n"
"ds_read_b128 v[20:23], v105\n"
"ds_read_b128 v[24:27], v106\n"
"s_mov_b64 exec, -1\n"
"v_mfma_f32_16x16x32_bf16 v[68:71], a[60:63], a[100:103], v[68:71]\n"
"ds_read_b128 a[16:19], v42 offset:24576\n"
"buffer_load_dwordx4 v[204:207], v2, s[20:23], s64 offen offset:1024 sc1\n"
"v_mfma_f32_16x16x32_bf16 v[88:91], a[64:67], a[96:99], v[88:91]\n"
"ds_read_b128 a[4:7], v41 offset:24576\n"
"s_cmp_eq_u32 s56, 6\n"
"s_cselect_b64 exec, -1, 0\n"
"ds_read_b64 v[28:29], v107\n"
"s_mov_b64 exec, -1\n"
"v_mfma_f32_16x16x32_bf16 v[88:91], a[68:71], a[100:103], v[88:91]\n"
"ds_read_b128 a[20:23], v43 offset:24576\n"
"v_mfma_f32_16x16x32_bf16 v[72:75], a[56:59], a[104:107], v[72:75]\n"
"ds_read_b128 a[8:11], v40 offset:26624\n"
"s_mul_i32 s75, s40, 5120\n"
"s_add_u32 m0, s75, 135296\n"
"s_mov_b32 s75, 0\n"
"buffer_load_dwordx4 v101, s[24:27], s75 offen offset:0  lds\n"
"v_mfma_f32_16x16x32_bf16 v[72:75], a[60:63], a[108:111], v[72:75]\n"
"ds_read_b128 a[12:15], v41 offset:26624\n"
"v_mfma_f32_16x16x32_bf16 v[92:95], a[64:67], a[104:107], v[92:95]\n"
"ds_read_b128 a[24:27], v42 offset:26624\n"
"v_mfma_f32_16x16x32_bf16 v[92:95], a[68:71], a[108:111], v[92:95]\n"
"ds_read_b128 a[28:31], v43 offset:26624\n"
"s_waitcnt lgkmcnt(4)\n"
"v_mfma_f32_16x16x32_bf16 v[56:59], a[0:3], a[16:19], v[56:59]\n"
"ds_read_b128 a[32:35], v42 offset:28672\n"
"v_mfma_f32_16x16x32_bf16 v[56:59], a[4:7], a[20:23], v[56:59]\n"
"ds_read_b128 a[36:39], v43 offset:28672\n"
"s_waitcnt lgkmcnt(4)\n"
"v_mfma_f32_16x16x32_bf16 v[76:79], a[8:11], a[16:19], v[76:79]\n"
"ds_read_b128 a[40:43], v42 offset:30720\n"
"s_addk_i32 s75, 26624\n"
"buffer_load_dwordx4 v101, s[24:27], s75 offen offset:1024  lds\n"
"v_mfma_f32_16x16x32_bf16 v[76:79], a[12:15], a[20:23], v[76:79]\n"
"ds_read_b128 a[44:47], v43 offset:30720\n"
"s_waitcnt lgkmcnt(4)\n"
"v_mfma_f32_16x16x32_bf16 v[60:63], a[0:3], a[24:27], v[60:63]\n"
"ds_read_b128 a[48:51], v113 offset:32768\n"
"v_mfma_f32_16x16x32_bf16 v[60:63], a[4:7], a[28:31], v[60:63]\n"
"ds_read_b128 a[52:55], v114 offset:32768\n"
"v_mfma_f32_16x16x32_bf16 v[80:83], a[8:11], a[24:27], v[80:83]\n"
"v_mfma_f32_16x16x32_bf16 v[80:83], a[12:15], a[28:31], v[80:83]\n"
"s_waitcnt lgkmcnt(4)\n"
"v_mfma_f32_16x16x32_bf16 v[64:67], a[0:3], a[32:35], v[64:67]\n"
"s_addk_i32 s75, 26624\n"
"buffer_load_dwordx4 v101, s[24:27], s75 offen offset:2048  lds\n"
"v_mfma_f32_16x16x32_bf16 v[64:67], a[4:7], a[36:39], v[64:67]\n"
"v_mfma_f32_16x16x32_bf16 v[84:87], a[8:11], a[32:35], v[84:87]\n"
"v_mfma_f32_16x16x32_bf16 v[84:87], a[12:15], a[36:39], v[84:87]\n"
"s_waitcnt vmcnt(19) lgkmcnt(0)\n"
"s_barrier\n"
"v_mfma_f32_16x16x32_bf16 v[68:71], a[0:3], a[40:43], v[68:71]\n"
"ds_read_b128 a[56:59], v40 offset:49152\n"
"s_cmp_eq_u32 s55, 7\n"
"s_cselect_b64 exec, -1, 0\n"
"ds_read_b128 v[20:23], v105\n"
"ds_read_b128 v[24:27], v106\n"
"s_mov_b64 exec, -1\n"
"v_mfma_f32_16x16x32_bf16 v[68:71], a[4:7], a[44:47], v[68:71]\n"
"ds_read_b128 a[72:75], v42 offset:49152\n"
"s_cmp_eq_u32 s56, 7\n"
"s_cselect_b64 exec, -1, 0\n"
"ds_read_b64 v[28:29], v107\n"
"s_mov_b64 exec, -1\n"
"v_mfma_f32_16x16x32_bf16 v[88:91], a[8:11], a[40:43], v[88:91]\n"
"ds_read_b128 a[60:63], v41 offset:49152\n"
"s_addk_i32 s75, 26624\n"
"buffer_load_dwordx4 v101, s[24:27], s75 offen offset:3072  lds\n"
"v_mfma_f32_16x16x32_bf16 v[88:91], a[12:15], a[44:47], v[88:91]\n"
"ds_read_b128 a[76:79], v43 offset:49152\n"
"v_mfma_f32_16x16x32_bf16 v[72:75], a[0:3], a[48:51], v[72:75]\n"
"ds_read_b128 a[64:67], v40 offset:51200\n"
"s_waitcnt vmcnt(6)\n"
"v_xor_b32 v46, s47, v161\n"
"v_mfma_f32_16x16x32_bf16 v[72:75], a[4:7], a[52:55], v[72:75]\n"
"ds_read_b128 a[68:71], v41 offset:51200\n"
"v_bitop3_b32 v46, v46, v163, s47 bitop3:0xf6\n"
"v_mfma_f32_16x16x32_bf16 v[92:95], a[8:11], a[48:51], v[92:95]\n"
"ds_read_b128 a[80:83], v42 offset:51200\n"
"v_cmp_ne_u32 vcc, 0, v46\n"
"s_cbranch_vccnz Lretry1_%=\n"
"Lback1_%=:\n"
"v_mfma_f32_16x16x32_bf16 v[92:95], a[12:15], a[52:55], v[92:95]\n"
"ds_read_b128 a[84:87], v43 offset:51200\n"
"s_waitcnt lgkmcnt(4)\n"
"v_mfma_f32_16x16x32_bf16 v[56:59], a[56:59], a[72:75], v[56:59]\n"
"ds_read_b128 a[88:91], v42 offset:53248\n"
"s_addk_i32 s75, 30720\n"
"s_add_u32 m0, m0, 4096\n"
"s_nop 0\n"
"buffer_load_dwordx4 v101, s[24:27], s75 offen offset:0  lds\n"
"v_mfma_f32_16x16x32_bf16 v[56:59], a[60:63], a[76:79], v[56:59]\n"
"ds_read_b128 a[92:95], v43 offset:53248\n"
"ds_write2_b32 v97, v160, v162 offset1:1\n"
"s_waitcnt lgkmcnt(5)\n"
"v_mfma_f32_16x16x32_bf16 v[76:79], a[64:67], a[72:75], v[76:79]\n"
"ds_read_b128 a[96:99], v42 offset:55296\n"
"v_and_or_b32 v164, v160, s4, v110\n"
"v_mfma_f32_16x16x32_bf16 v[76:79], a[68:71], a[76:79], v[76:79]\n"
"ds_read_b128 a[100:103], v43 offset:55296\n"
"v_and_or_b32 v165, v162, s4, v110\n"
"s_waitcnt lgkmcnt(5)\n"
"v_mfma_f32_16x16x32_bf16 v[60:63], a[56:59], a[80:83], v[60:63]\n"
"ds_read_b128 a[104:107], v113 offset:34816\n"
"v_rcp_f32 v164, v164\n"
"v_mfma_f32_16x16x32_bf16 v[60:63], a[60:63], a[84:87], v[60:63]\n"
"ds_read_b128 a[108:111], v114 offset:34816\n"
"v_rcp_f32 v165, v165\n"
"v_mfma_f32_16x16x32_bf16 v[80:83], a[64:67], a[80:83], v[80:83]\n"
"buffer_load_dwordx4 a[236:239], v102, s[28:31], 0 offen\n"
"v_mfma_f32_16x16x32_bf16 v[80:83], a[68:71], a[84:87], v[80:83]\n"
"v_lshlrev_b32 v166, 16, v160\n"
"v_lshlrev_b32 v167, 16, v162\n"
"s_waitcnt lgkmcnt(5)\n"
"v_mfma_f32_16x16x32_bf16 v[64:67], a[56:59], a[88:91], v[64:67]\n"
"ds_write_b64 v97, v[164:165] offset:512\n"
"ds_write_b64 v97, v[166:167] offset:1024\n"
"v_mfma_f32_16x16x32_bf16 v[64:67], a[60:63], a[92:95], v[64:67]\n"
"v_mfma_f32_16x16x32_bf16 v[84:87], a[64:67], a[88:91], v[84:87]\n"
"ds_read2_b32 v[152:153], v98 offset1:4\n"
"ds_read_b128 v[144:147], v100 offset:1408\n"
"v_mfma_f32_16x16x32_bf16 v[84:87], a[68:71], a[92:95], v[84:87]\n"
"ds_read_b128 v[148:151], v100 offset:1424\n"
"ds_read_b128 v[136:139], v100 offset:896\n"
"s_waitcnt lgkmcnt(6)\n"
"s_barrier\n"
"v_mfma_f32_16x16x32_bf16 v[68:71], a[56:59], a[96:99], v[68:71]\n"
"ds_read_b128 a[0:3], v111 offset:0\n"
"buffer_load_dwordx4 a[240:243], v103, s[28:31], 0 offen\n"
"v_mfma_f32_16x16x32_bf16 v[68:71], a[60:63], a[100:103], v[68:71]\n"
"ds_read_b128 a[16:19], v113 offset:0\n"
"s_cmp_eq_u32 s55, 8\n"
"s_cselect_b64 exec, -1, 0\n"
"ds_read_b128 v[20:23], v105\n"
"ds_read_b128 v[24:27], v106\n"
"s_mov_b64 exec, -1\n"
"v_mfma_f32_16x16x32_bf16 v[88:91], a[64:67], a[96:99], v[88:91]\n"
"ds_read_b128 a[4:7], v112 offset:0\n"
"s_cmp_eq_u32 s56, 8\n"
"s_cselect_b64 exec, -1, 0\n"
"ds_read_b64 v[28:29], v107\n"
"s_mov_b64 exec, -1\n"
"v_mfma_f32_16x16x32_bf16 v[88:91], a[68:71], a[100:103], v[88:91]\n"
"ds_read_b128 a[20:23], v114 offset:0\n"
"ds_read_b128 v[140:143], v100 offset:912\n"
"v_mfma_f32_16x16x32_bf16 v[72:75], a[56:59], a[104:107], v[72:75]\n"
"ds_read_b128 a[8:11], v111 offset:2048\n"
"ds_read2_b32 v[154:155], v98 offset0:32 offset1:36\n"
"v_mfma_f32_16x16x32_bf16 v[72:75], a[60:63], a[108:111], v[72:75]\n"
"ds_read_b128 a[12:15], v112 offset:2048\n"
"ds_read_b32 v156, v99 offset:256\n"
"v_mfma_f32_16x16x32_bf16 v[92:95], a[64:67], a[104:107], v[92:95]\n"
"ds_read_b128 a[24:27], v113 offset:2048\n"
"buffer_load_dwordx4 a[244:247], v104, s[28:31], 0 offen\n"
"v_mfma_f32_16x16x32_bf16 v[92:95], a[68:71], a[108:111], v[92:95]\n"
"ds_read_b128 a[28:31], v114 offset:2048\n"
"s_waitcnt lgkmcnt(7)\n"
"v_mfma_f32_16x16x32_bf16 v[56:59], a[0:3], a[16:19], v[56:59]\n"
"ds_read_b128 a[32:35], v113 offset:4096\n"
"s_lshl_b32 s66, s40, 9\n"
"v_lshl_add_u32 v109, v1, 2, s66\n"
"v_mfma_f32_16x16x32_bf16 v[56:59], a[4:7], a[20:23], v[56:59]\n"
"ds_read_b128 a[36:39], v114 offset:4096\n"
"v_xor_b32 v109, 0x7ff, v109\n"
"s_waitcnt lgkmcnt(5)\n"
"v_mfma_f32_16x16x32_bf16 v[76:79], a[8:11], a[16:19], v[76:79]\n"
"ds_read_b128 a[40:43], v113 offset:6144\n"
"s_waitcnt vmcnt(8)\n"
"v_xor_b32 v46, s47, v201\n"
"v_mfma_f32_16x16x32_bf16 v[76:79], a[12:15], a[20:23], v[76:79]\n"
"ds_read_b128 a[44:47], v114 offset:6144\n"
"v_bitop3_b32 v46, v46, v203, s47 bitop3:0xf6\n"
"s_waitcnt lgkmcnt(4)\n"
"v_mfma_f32_16x16x32_bf16 v[60:63], a[0:3], a[24:27], v[60:63]\n"
"ds_read_b128 a[48:51], v113 offset:36864\n"
"s_bfe_u32 s75, s49, 0x40000\n"
"s_lshl_b32 s75, s75, 21\n"
"s_add_u32 s75, s75, s50\n"
"s_add_u32 m0, s54, 0\n"
"s_nop 0\n"
"buffer_load_dwordx4 v3, s[16:19], s75 offen offset:0  lds\n"
"v_mfma_f32_16x16x32_bf16 v[60:63], a[4:7], a[28:31], v[60:63]\n"
"ds_read_b128 a[52:55], v114 offset:36864\n"
"v_bitop3_b32 v46, v46, v205, s47 bitop3:0xf6\n"
"v_mfma_f32_16x16x32_bf16 v[80:83], a[8:11], a[24:27], v[80:83]\n"
"v_bitop3_b32 v46, v46, v207, s47 bitop3:0xf6\n"
"v_cmp_ne_u32 vcc, 0, v46\n"
"s_cbranch_vccnz Lretry0_%=\n"
"Lback0_%=:\n"
"v_mfma_f32_16x16x32_bf16 v[80:83], a[12:15], a[28:31], v[80:83]\n"
"v_lshl_or_b32 v44, v200, 16, v109\n"
"v_and_or_b32 v46, v200, s4, v109\n"
"s_waitcnt lgkmcnt(4)\n"
"v_mfma_f32_16x16x32_bf16 v[64:67], a[0:3], a[32:35], v[64:67]\n"
"v_xor_b32 v46, 1, v46\n"
"v_min_f32 v45, v44, v46\n"
"v_mfma_f32_16x16x32_bf16 v[64:67], a[4:7], a[36:39], v[64:67]\n"
"v_max_f32 v44, v44, v46\n"
"v_lshl_or_b32 v46, v202, 16, v109\n"
"v_mfma_f32_16x16x32_bf16 v[84:87], a[8:11], a[32:35], v[84:87]\n"
"buffer_load_dwordx4 v4, s[16:19], s75 offen offset:1056  lds\n"
"v_mfma_f32_16x16x32_bf16 v[84:87], a[12:15], a[36:39], v[84:87]\n"
"v_xor_b32 v46, 2, v46\n"
"v_med3_f32 v45, v44, v45, v46\n"
"v_max_f32 v44, v44, v46\n"
"v_and_or_b32 v46, v202, s4, v109\n"
"v_xor_b32 v46, 3, v46\n"
"v_med3_f32 v45, v44, v45, v46\n"
"v_max_f32 v44, v44, v46\n"
"v_lshl_or_b32 v46, v204, 16, v109\n"
"v_xor_b32 v46, 256, v46\n"
"v_med3_f32 v45, v44, v45, v46\n"
"v_max_f32 v44, v44, v46\n"
"v_and_or_b32 v46, v204, s4, v109\n"
"v_xor_b32 v46, 257, v46\n"
"v_med3_f32 v45, v44, v45, v46\n"
"v_max_f32 v44, v44, v46\n"
"v_lshl_or_b32 v46, v206, 16, v109\n"
"v_xor_b32 v46, 258, v46\n"
"v_med3_f32 v45, v44, v45, v46\n"
"v_max_f32 v44, v44, v46\n"
"v_and_or_b32 v46, v206, s4, v109\n"
"v_xor_b32 v46, 259, v46\n"
"v_med3_f32 v45, v44, v45, v46\n"
"v_max_f32 v44, v44, v46\n"
"s_nop 1\n"
"v_min_f32_dpp v46, v44, v44 quad_perm:[1,0,3,2] row_mask:0xf bank_mask:0xf\n"
"v_max_f32_dpp v45, v45, v45 quad_perm:[1,0,3,2] row_mask:0xf bank_mask:0xf\n"
"v_max_f32_dpp v44, v44, v44 quad_perm:[1,0,3,2] row_mask:0xf bank_mask:0xf\n"
"v_max_f32 v45, v45, v46\n"
"s_nop 0\n"
"v_min_f32_dpp v46, v44, v44 quad_perm:[2,3,0,1] row_mask:0xf bank_mask:0xf\n"
"v_max_f32_dpp v45, v45, v45 quad_perm:[2,3,0,1] row_mask:0xf bank_mask:0xf\n"
"v_max_f32_dpp v44, v44, v44 quad_perm:[2,3,0,1] row_mask:0xf bank_mask:0xf\n"
"v_max_f32 v45, v45, v46\n"
"s_nop 0\n"
"v_min_f32_dpp v46, v44, v44 row_half_mirror row_mask:0xf bank_mask:0xf\n"
"v_max_f32_dpp v45, v45, v45 row_half_mirror row_mask:0xf bank_mask:0xf\n"
"v_max_f32_dpp v44, v44, v44 row_half_mirror row_mask:0xf bank_mask:0xf\n"
"v_max_f32 v45, v45, v46\n"
"s_nop 0\n"
"v_min_f32_dpp v46, v44, v44 row_mirror row_mask:0xf bank_mask:0xf\n"
"v_max_f32_dpp v45, v45, v45 row_mirror row_mask:0xf bank_mask:0xf\n"
"v_max_f32_dpp v44, v44, v44 row_mirror row_mask:0xf bank_mask:0xf\n"
"v_max_f32 v45, v45, v46\n"
"v_lshrrev_b32 v46, 4, v0\n"
"v_lshlrev_b32 v46, 3, v46\n"
"v_add_u32 v46, 161920, v46\n"
"ds_write_b64 v46, v[44:45]\n"
"v_and_b32 v46, 15, v0\n"
"v_lshlrev_b32 v46, 3, v46\n"
"v_add_u32 v46, 161920, v46\n"
"s_waitcnt lgkmcnt(0)\n"
"s_barrier\n"
"v_mfma_f32_16x16x32_bf16 v[68:71], a[0:3], a[40:43], v[68:71]\n"
"ds_read_b128 a[56:59], v111 offset:24576\n"
"s_cmp_eq_u32 s55, 9\n"
"s_cselect_b64 exec, -1, 0\n"
"ds_read_b128 v[20:23], v105\n"
"ds_read_b128 v[24:27], v106\n"
"s_mov_b64 exec, -1\n"
"v_mfma_f32_16x16x32_bf16 v[68:71], a[4:7], a[44:47], v[68:71]\n"
"ds_read_b128 a[72:75], v113 offset:24576\n"
"s_cmp_eq_u32 s56, 9\n"
"s_cselect_b64 exec, -1, 0\n"
"ds_read_b64 v[28:29], v107\n"
"s_mov_b64 exec, -1\n"
"v_mfma_f32_16x16x32_bf16 v[88:91], a[8:11], a[40:43], v[88:91]\n"
"ds_read_b128 a[60:63], v112 offset:24576\n"
"ds_read_b64 v[44:45], v46\n"
"v_mfma_f32_16x16x32_bf16 v[88:91], a[12:15], a[44:47], v[88:91]\n"
"ds_read_b128 a[76:79], v114 offset:24576\n"
"s_mul_i32 s60, s72, 160\n"
"s_movk_i32 s61, 4096\n"
"v_mfma_f32_16x16x32_bf16 v[72:75], a[0:3], a[48:51], v[72:75]\n"
"ds_read_b128 a[64:67], v111 offset:26624\n"
"buffer_load_dwordx4 v5, s[16:19], s75 offen offset:2112  lds\n"
"v_mfma_f32_16x16x32_bf16 v[72:75], a[4:7], a[52:55], v[72:75]\n"
"ds_read_b128 a[68:71], v112 offset:26624\n"
"v_lshrrev_b32 v47, 4, v1\n"
"v_mfma_f32_16x16x32_bf16 v[92:95], a[8:11], a[48:51], v[92:95]\n"
"ds_read_b128 a[80:83], v113 offset:26624\n"
"v_mul_u32_u24 v47, 40, v47\n"
"v_mfma_f32_16x16x32_bf16 v[92:95], a[12:15], a[52:55], v[92:95]\n"
"ds_read_b128 a[84:87], v114 offset:26624\n"
"v_and_b32 v48, 1, v0\n"
"s_waitcnt lgkmcnt(4)\n"
"v_mfma_f32_16x16x32_bf16 v[56:59], a[56:59], a[72:75], v[56:59]\n"
"ds_read_b128 a[88:91], v113 offset:28672\n"
"v_mfma_f32_16x16x32_bf16 v[56:59], a[60:63], a[76:79], v[56:59]\n"
"ds_read_b128 a[92:95], v114 offset:28672\n"
"v_min_f32_dpp v46, v44, v44 quad_perm:[1,0,3,2] row_mask:0xf bank_mask:0xf\n"
"s_waitcnt lgkmcnt(4)\n"
"v_mfma_f32_16x16x32_bf16 v[76:79], a[64:67], a[72:75], v[76:79]\n"
"ds_read_b128 a[96:99], v113 offset:30720\n"
"buffer_load_dwordx4 v6, s[16:19], s75 offen offset:3168  lds\n"
"v_mfma_f32_16x16x32_bf16 v[76:79], a[68:71], a[76:79], v[76:79]\n"
"ds_read_b128 a[100:103], v114 offset:30720\n"
"v_max_f32_dpp v45, v45, v45 quad_perm:[1,0,3,2] row_mask:0xf bank_mask:0xf\n"
"s_waitcnt lgkmcnt(4)\n"
"v_mfma_f32_16x16x32_bf16 v[60:63], a[56:59], a[80:83], v[60:63]\n"
"ds_read_b128 a[104:107], v113 offset:38912\n"
"v_max_f32_dpp v44, v44, v44 quad_perm:[1,0,3,2] row_mask:0xf bank_mask:0xf\n"
"v_mfma_f32_16x16x32_bf16 v[60:63], a[60:63], a[84:87], v[60:63]\n"
"ds_read_b128 a[108:111], v114 offset:38912\n"
"v_max_f32 v45, v45, v46\n"
"v_mfma_f32_16x16x32_bf16 v[80:83], a[64:67], a[80:83], v[80:83]\n"
"v_min_f32_dpp v46, v44, v44 quad_perm:[2,3,0,1] row_mask:0xf bank_mask:0xf\n"
"v_max_f32_dpp v45, v45, v45 quad_perm:[2,3,0,1] row_mask:0xf bank_mask:0xf\n"
"v_mfma_f32_16x16x32_bf16 v[80:83], a[68:71], a[84:87], v[80:83]\n"
"v_max_f32_dpp v44, v44, v44 quad_perm:[2,3,0,1] row_mask:0xf bank_mask:0xf\n"
"v_max_f32 v45, v45, v46\n"
"s_waitcnt lgkmcnt(4)\n"
"v_mfma_f32_16x16x32_bf16 v[64:67], a[56:59], a[88:91], v[64:67]\n"
"s_add_u32 m0, s54, 4224\n"
"s_addk_i32 s75, 0x4000\n"
"buffer_load_dwordx4 v3, s[16:19], s75 offen offset:0  lds\n"
"v_mfma_f32_16x16x32_bf16 v[64:67], a[60:63], a[92:95], v[64:67]\n"
"v_min_f32_dpp v46, v44, v44 row_half_mirror row_mask:0xf bank_mask:0xf\n"
"v_max_f32_dpp v45, v45, v45 row_half_mirror row_mask:0xf bank_mask:0xf\n"
"v_mfma_f32_16x16x32_bf16 v[84:87], a[64:67], a[88:91], v[84:87]\n"
"v_max_f32_dpp v44, v44, v44 row_half_mirror row_mask:0xf bank_mask:0xf\n"
"v_max_f32 v45, v45, v46\n"
"v_mfma_f32_16x16x32_bf16 v[84:87], a[68:71], a[92:95], v[84:87]\n"
"v_min_f32_dpp v46, v44, v44 row_mirror row_mask:0xf bank_mask:0xf\n"
"v_max_f32_dpp v45, v45, v45 row_mirror row_mask:0xf bank_mask:0xf\n"
"s_waitcnt lgkmcnt(2)\n"
"v_mfma_f32_16x16x32_bf16 v[68:71], a[56:59], a[96:99], v[68:71]\n"
"v_max_f32_dpp v44, v44, v44 row_mirror row_mask:0xf bank_mask:0xf\n"
"v_max_f32 v45, v45, v46\n"
"v_mfma_f32_16x16x32_bf16 v[68:71], a[60:63], a[100:103], v[68:71]\n"
"v_readfirstlane_b32 s57, v44\n"
"v_readfirstlane_b32 s58, v45\n"
"v_mfma_f32_16x16x32_bf16 v[88:91], a[64:67], a[96:99], v[88:91]\n"
"buffer_load_dwordx4 v4, s[16:19], s75 offen offset:1056  lds\n"
"v_mfma_f32_16x16x32_bf16 v[88:91], a[68:71], a[100:103], v[88:91]\n"
"s_xor_b32 s57, s57, 0x7ff\n"
"s_xor_b32 s58, s58, 0x7ff\n"
"s_and_b32 s55, s57, 0x7ff\n"
"s_and_b32 s56, s58, 0x7ff\n"
"s_sub_u32 s66, s56, s55\n"
"s_waitcnt lgkmcnt(0)\n"
"v_mfma_f32_16x16x32_bf16 v[72:75], a[56:59], a[104:107], v[72:75]\n"
"v_mul_i32_i24 v44, s66, v48\n"
"v_add_u32 v44, s55, v44\n"
"v_mfma_f32_16x16x32_bf16 v[72:75], a[60:63], a[108:111], v[72:75]\n"
"v_mad_u32_u24 v116, v44, s67, v47\n"
"v_lshl_add_u32 v117, v44, 3, s61\n"
"v_mfma_f32_16x16x32_bf16 v[92:95], a[64:67], a[104:107], v[92:95]\n"
"s_mov_b32 exec_lo, 0x30003\n"
"s_mov_b32 exec_hi, 0x30003\n"
"buffer_load_dwordx4 v[212:215], v116, s[8:11], s60 offen\n"
"buffer_load_dwordx4 v[216:219], v116, s[8:11], s60 offen offset:16\n"
"buffer_load_dwordx2 v[220:221], v116, s[8:11], s60 offen offset:32\n"
"s_mov_b64 exec, -1\n"
"v_mfma_f32_16x16x32_bf16 v[92:95], a[68:71], a[108:111], v[92:95]\n"
"s_sub_u32 s66, s71, 27\n"
"s_lshl3_add_u32 s65, s66, s72\n"
"s_cmp_eq_u32 s40, 0\n"
"s_cselect_b32 s57, s57, s58\n"
"s_mul_i32 s58, s40, 40\n"
"s_add_u32 s58, s58, s65\n"
"s_and_b32 s66, s57, 0x7ff\n"
"s_mulk_i32 s66, 40\n"
"s_add_u32 s66, s66, s65\n"
"s_lshl_b32 s66, s66, 5\n"
"s_cmp_lt_u32 s40, 2\n"
"s_cselect_b32 s80, s71, 0\n"
"v_and_b32 v44, 7, v0\n"
"v_lshlrev_b32 v44, 2, v44\n"
"s_cmp_ge_u32 s80, 27\n"
"s_cselect_b64 s[36:37], -1, 1\n"
"s_cselect_b64 s[38:39], -1, 0\n"
"s_cselect_b32 s66, s66, 1073741824\n"
"s_mov_b64 exec, s[36:37]\n"
"buffer_load_dword v224, v44, s[8:11], s66 offen\n"
"s_mov_b64 exec, -1\n"
"buffer_load_dwordx2 v[222:223], v117, s[20:23], 0 offen sc1\n"
"v_pk_fma_f32 v[56:57], v[144:145], v[152:153], v[56:57] op_sel_hi:[1,0,1] neg_lo:[1,0,0] neg_hi:[1,0,0]\n"
"v_pk_fma_f32 v[58:59], v[146:147], v[152:153], v[58:59] op_sel_hi:[1,0,1] neg_lo:[1,0,0] neg_hi:[1,0,0]\n"
"v_pk_fma_f32 v[76:77], v[148:149], v[152:153], v[76:77] op_sel_hi:[1,0,1] neg_lo:[1,0,0] neg_hi:[1,0,0]\n"
"v_pk_fma_f32 v[78:79], v[150:151], v[152:153], v[78:79] op_sel_hi:[1,0,1] neg_lo:[1,0,0] neg_hi:[1,0,0]\n"
"v_pk_mul_f32 v[56:57], v[56:57], v[136:137]\n"
"v_pk_mul_f32 v[58:59], v[58:59], v[138:139]\n"
"v_pk_mul_f32 v[76:77], v[76:77], v[140:141]\n"
"v_pk_mul_f32 v[78:79], v[78:79], v[142:143]\n"
"v_cvt_pk_bf16_f32 v176, v56, v57\n"
"v_cvt_pk_bf16_f32 v177, v58, v59\n"
"v_cvt_pk_bf16_f32 v178, v76, v77\n"
"v_cvt_pk_bf16_f32 v179, v78, v79\n"
"buffer_store_dwordx4 v[176:179], v2, s[20:23], s77 offen offset:0 sc1\n"
"v_pk_fma_f32 v[60:61], v[144:145], v[152:153], v[60:61] op_sel:[0,1,0] neg_lo:[1,0,0] neg_hi:[1,0,0]\n"
"v_pk_fma_f32 v[62:63], v[146:147], v[152:153], v[62:63] op_sel:[0,1,0] neg_lo:[1,0,0] neg_hi:[1,0,0]\n"
"v_pk_fma_f32 v[80:81], v[148:149], v[152:153], v[80:81] op_sel:[0,1,0] neg_lo:[1,0,0] neg_hi:[1,0,0]\n"
"v_pk_fma_f32 v[82:83], v[150:151], v[152:153], v[82:83] op_sel:[0,1,0] neg_lo:[1,0,0] neg_hi:[1,0,0]\n"
"v_pk_mul_f32 v[60:61], v[60:61], v[136:137]\n"
"v_pk_mul_f32 v[62:63], v[62:63], v[138:139]\n"
"v_pk_mul_f32 v[80:81], v[80:81], v[140:141]\n"
"v_pk_mul_f32 v[82:83], v[82:83], v[142:143]\n"
"v_cvt_pk_bf16_f32 v180, v60, v61\n"
"v_cvt_pk_bf16_f32 v181, v62, v63\n"
"v_cvt_pk_bf16_f32 v182, v80, v81\n"
"v_cvt_pk_bf16_f32 v183, v82, v83\n"
"buffer_store_dwordx4 v[180:183], v2, s[20:23], s77 offen offset:1024 sc1\n"
"v_pk_fma_f32 v[64:65], v[144:145], v[154:155], v[64:65] op_sel_hi:[1,0,1] neg_lo:[1,0,0] neg_hi:[1,0,0]\n"
"v_pk_fma_f32 v[66:67], v[146:147], v[154:155], v[66:67] op_sel_hi:[1,0,1] neg_lo:[1,0,0] neg_hi:[1,0,0]\n"
"v_pk_fma_f32 v[84:85], v[148:149], v[154:155], v[84:85] op_sel_hi:[1,0,1] neg_lo:[1,0,0] neg_hi:[1,0,0]\n"
"v_pk_fma_f32 v[86:87], v[150:151], v[154:155], v[86:87] op_sel_hi:[1,0,1] neg_lo:[1,0,0] neg_hi:[1,0,0]\n"
"v_pk_mul_f32 v[64:65], v[64:65], v[136:137]\n"
"v_pk_mul_f32 v[66:67], v[66:67], v[138:139]\n"
"v_pk_mul_f32 v[84:85], v[84:85], v[140:141]\n"
"v_pk_mul_f32 v[86:87], v[86:87], v[142:143]\n"
"v_cvt_pk_bf16_f32 v184, v64, v65\n"
"v_cvt_pk_bf16_f32 v185, v66, v67\n"
"v_cvt_pk_bf16_f32 v186, v84, v85\n"
"v_cvt_pk_bf16_f32 v187, v86, v87\n"
"buffer_store_dwordx4 v[184:187], v2, s[20:23], s77 offen offset:2048 sc1\n"
"v_pk_fma_f32 v[68:69], v[144:145], v[154:155], v[68:69] op_sel:[0,1,0] neg_lo:[1,0,0] neg_hi:[1,0,0]\n"
"v_pk_fma_f32 v[70:71], v[146:147], v[154:155], v[70:71] op_sel:[0,1,0] neg_lo:[1,0,0] neg_hi:[1,0,0]\n"
"v_pk_fma_f32 v[88:89], v[148:149], v[154:155], v[88:89] op_sel:[0,1,0] neg_lo:[1,0,0] neg_hi:[1,0,0]\n"
"v_pk_fma_f32 v[90:91], v[150:151], v[154:155], v[90:91] op_sel:[0,1,0] neg_lo:[1,0,0] neg_hi:[1,0,0]\n"
"v_pk_mul_f32 v[68:69], v[68:69], v[136:137]\n"
"v_pk_mul_f32 v[70:71], v[70:71], v[138:139]\n"
"v_pk_mul_f32 v[88:89], v[88:89], v[140:141]\n"
"v_pk_mul_f32 v[90:91], v[90:91], v[142:143]\n"
"v_cvt_pk_bf16_f32 v188, v68, v69\n"
"v_cvt_pk_bf16_f32 v189, v70, v71\n"
"v_cvt_pk_bf16_f32 v190, v88, v89\n"
"v_cvt_pk_bf16_f32 v191, v90, v91\n"
"buffer_store_dwordx4 v[188:191], v2, s[20:23], s77 offen offset:3072 sc1\n"
"v_pk_fma_f32 v[72:73], v[144:145], v[156:157], v[72:73] op_sel_hi:[1,0,1] neg_lo:[1,0,0] neg_hi:[1,0,0]\n"
"v_pk_fma_f32 v[74:75], v[146:147], v[156:157], v[74:75] op_sel_hi:[1,0,1] neg_lo:[1,0,0] neg_hi:[1,0,0]\n"
"v_pk_fma_f32 v[92:93], v[148:149], v[156:157], v[92:93] op_sel_hi:[1,0,1] neg_lo:[1,0,0] neg_hi:[1,0,0]\n"
"v_pk_fma_f32 v[94:95], v[150:151], v[156:157], v[94:95] op_sel_hi:[1,0,1] neg_lo:[1,0,0] neg_hi:[1,0,0]\n"
"v_pk_mul_f32 v[72:73], v[72:73], v[136:137]\n"
"v_pk_mul_f32 v[74:75], v[74:75], v[138:139]\n"
"v_pk_mul_f32 v[92:93], v[92:93], v[140:141]\n"
"v_pk_mul_f32 v[94:95], v[94:95], v[142:143]\n"
"v_cvt_pk_bf16_f32 v192, v72, v73\n"
"v_cvt_pk_bf16_f32 v193, v74, v75\n"
"v_cvt_pk_bf16_f32 v194, v92, v93\n"
"v_cvt_pk_bf16_f32 v195, v94, v95\n"
"buffer_store_dwordx4 v[192:195], v2, s[20:23], s76 offen offset:0 sc1\n"
"buffer_load_dwordx4 v5, s[16:19], s75 offen offset:2112  lds\n"
"v_lshrrev_b32 v48, 4, v1\n"
"v_lshrrev_b32 v49, 2, v1\n"
"v_and_b32 v50, 3, v0\n"
"v_lshlrev_b32 v50, 3, v50\n"
"s_lshl_b32 s66, s40, 5\n"
"s_add_u32 s66, s66, 135296\n"
"v_lshl_add_u32 v49, v48, 4, v49\n"
"v_lshl_add_u32 v115, v49, 7, v50\n"
"v_add_u32 v115, s66, v115\n"
"s_lshl_b32 s81, s71, 13\n"
"s_lshl_b32 s66, s40, 8\n"
"s_add_u32 s81, s81, s66\n"
"s_add_u32 s81, s81, 65536\n"
"s_xor_b32 s66, s43, 1\n"
"s_lshl3_add_u32 s66, s66, 8\n"
"s_load_dwordx2 s[12:13], s[0:1], s66\n"
"s_mov_b32 s14, 8388608\n"
"s_waitcnt vmcnt(17)\n"
"s_waitcnt lgkmcnt(0)\n"
"s_barrier\n"
"ds_read_b64_tr_b16 v[56:57], v115 offset:0\n"
"ds_read_b64_tr_b16 v[58:59], v115 offset:512\n"
"ds_read_b64_tr_b16 v[60:61], v115 offset:1024\n"
"ds_read_b64_tr_b16 v[62:63], v115 offset:1536\n"
"ds_read_b64_tr_b16 v[64:65], v115 offset:2048\n"
"ds_read_b64_tr_b16 v[66:67], v115 offset:10240\n"
"buffer_load_dwordx4 v6, s[16:19], s75 offen offset:3168  lds\n"
"ds_read_b64_tr_b16 v[68:69], v115 offset:10752\n"
"ds_read_b64_tr_b16 v[70:71], v115 offset:11264\n"
"ds_read_b64_tr_b16 v[72:73], v115 offset:11776\n"
"ds_read_b64_tr_b16 v[74:75], v115 offset:12288\n"
"v_xor_b32 v44, 64, v108\n"
"ds_read2_b32 v[208:209], v108 offset1:128\n"
"ds_read2_b32 v[210:211], v44 offset1:128\n"
"s_or_b32 s66, s2, s40\n"
"s_cmp_eq_u32 s66, 0\n"
"s_cbranch_scc1 Lrare0_%=\n"
"Lrareb0_%=:\n"
"v_cmp_lt_u32 vcc, 15, v1\n"
"v_mov_b32 v44, 536870912\n"
"v_mov_b32 v54, s3\n"
"v_cndmask_b32 v55, v2, v44, vcc\n"
"s_waitcnt vmcnt(7)\n"
"v_cmp_ne_u32 vcc, s47, v223\n"
"s_cbranch_vccnz Lretry2_%=\n"
"Lback2_%=:\n"
"v_and_b32 v223, s4, v222\n"
"v_rcp_f32 v223, v223\n"
"v_lshlrev_b32 v222, 16, v222\n"
"v_lshlrev_b32 v120, 16, v212\n"
"v_lshlrev_b32 v122, 16, v213\n"
"v_and_b32 v121, s4, v212\n"
"s_bfe_u32 s75, s49, 0x40000\n"
"s_lshl_b32 s75, s75, 21\n"
"s_add_u32 s75, s75, s50\n"
"s_add_u32 m0, s54, 33792\n"
"s_nop 0\n"
"buffer_load_dwordx4 v3, s[12:15], s75 offen offset:0  lds\n"
"v_and_b32 v123, s4, v213\n"
"v_pk_add_f32 v[120:121], v[120:121], v[222:223] op_sel_hi:[1,0] neg_lo:[0,1] neg_hi:[0,1]\n"
"v_pk_add_f32 v[122:123], v[122:123], v[222:223] op_sel_hi:[1,0] neg_lo:[0,1] neg_hi:[0,1]\n"
"v_cvt_pk_bf16_f32 v120, v120, v121\n"
"v_cvt_pk_bf16_f32 v122, v122, v123\n"
"v_and_b32 v121, s4, v120\n"
"v_and_b32 v123, s4, v122\n"
"v_lshlrev_b32 v120, 16, v120\n"
"v_lshlrev_b32 v122, 16, v122\n"
"v_pk_mul_f32 v[120:121], v[120:121], v[222:223] op_sel:[0,1]\n"
"v_pk_mul_f32 v[122:123], v[122:123], v[222:223] op_sel:[0,1]\n"
"v_cvt_pk_bf16_f32 v212, v120, v121\n"
"v_cvt_pk_bf16_f32 v213, v122, v123\n"
"v_lshlrev_b32 v120, 16, v214\n"
"v_lshlrev_b32 v122, 16, v215\n"
"v_and_b32 v121, s4, v214\n"
"v_and_b32 v123, s4, v215\n"
"v_pk_add_f32 v[120:121], v[120:121], v[222:223] op_sel_hi:[1,0] neg_lo:[0,1] neg_hi:[0,1]\n"
"v_pk_add_f32 v[122:123], v[122:123], v[222:223] op_sel_hi:[1,0] neg_lo:[0,1] neg_hi:[0,1]\n"
"v_cvt_pk_bf16_f32 v120, v120, v121\n"
"buffer_load_dwordx4 v4, s[12:15], s75 offen offset:1056  lds\n"
"v_cvt_pk_bf16_f32 v122, v122, v123\n"
"v_and_b32 v121, s4, v120\n"
"v_and_b32 v123, s4, v122\n"
"v_lshlrev_b32 v120, 16, v120\n"
"v_lshlrev_b32 v122, 16, v122\n"
"v_pk_mul_f32 v[120:121], v[120:121], v[222:223] op_sel:[0,1]\n"
"v_pk_mul_f32 v[122:123], v[122:123], v[222:223] op_sel:[0,1]\n"
"v_cvt_pk_bf16_f32 v214, v120, v121\n"
"v_cvt_pk_bf16_f32 v215, v122, v123\n"
"v_lshlrev_b32 v120, 16, v216\n"
"v_lshlrev_b32 v122, 16, v217\n"
"v_and_b32 v121, s4, v216\n"
"v_and_b32 v123, s4, v217\n"
"v_pk_add_f32 v[120:121], v[120:121], v[222:223] op_sel_hi:[1,0] neg_lo:[0,1] neg_hi:[0,1]\n"
"v_pk_add_f32 v[122:123], v[122:123], v[222:223] op_sel_hi:[1,0] neg_lo:[0,1] neg_hi:[0,1]\n"
"v_cvt_pk_bf16_f32 v120, v120, v121\n"
"v_cvt_pk_bf16_f32 v122, v122, v123\n"
"v_and_b32 v121, s4, v120\n"
"v_and_b32 v123, s4, v122\n"
"v_lshlrev_b32 v120, 16, v120\n"
"v_lshlrev_b32 v122, 16, v122\n"
"v_pk_mul_f32 v[120:121], v[120:121], v[222:223] op_sel:[0,1]\n"
"v_pk_mul_f32 v[122:123], v[122:123], v[222:223] op_sel:[0,1]\n"
"v_cvt_pk_bf16_f32 v216, v120, v121\n"
"v_cvt_pk_bf16_f32 v217, v122, v123\n"
"v_lshlrev_b32 v120, 16, v218\n"
"v_lshlrev_b32 v122, 16, v219\n"
"v_and_b32 v121, s4, v218\n"
"v_and_b32 v123, s4, v219\n"
"v_pk_add_f32 v[120:121], v[120:121], v[222:223] op_sel_hi:[1,0] neg_lo:[0,1] neg_hi:[0,1]\n"
"v_pk_add_f32 v[122:123], v[122:123], v[222:223] op_sel_hi:[1,0] neg_lo:[0,1] neg_hi:[0,1]\n"
"v_cvt_pk_bf16_f32 v120, v120, v121\n"
"v_cvt_pk_bf16_f32 v122, v122, v123\n"
"v_and_b32 v121, s4, v120\n"
"v_and_b32 v123, s4, v122\n"
"v_lshlrev_b32 v120, 16, v120\n"
"v_lshlrev_b32 v122, 16, v122\n"
"v_pk_mul_f32 v[120:121], v[120:121], v[222:223] op_sel:[0,1]\n"
"v_pk_mul_f32 v[122:123], v[122:123], v[222:223] op_sel:[0,1]\n"
"v_cvt_pk_bf16_f32 v218, v120, v121\n"
"v_cvt_pk_bf16_f32 v219, v122, v123\n"
"v_lshlrev_b32 v120, 16, v220\n"
"v_lshlrev_b32 v122, 16, v221\n"
"v_and_b32 v121, s4, v220\n"
"v_and_b32 v123, s4, v221\n"
"v_pk_add_f32 v[120:121], v[120:121], v[222:223] op_sel_hi:[1,0] neg_lo:[0,1] neg_hi:[0,1]\n"
"v_pk_add_f32 v[122:123], v[122:123], v[222:223] op_sel_hi:[1,0] neg_lo:[0,1] neg_hi:[0,1]\n"
"v_cvt_pk_bf16_f32 v120, v120, v121\n"
"v_cvt_pk_bf16_f32 v122, v122, v123\n"
"v_and_b32 v121, s4, v120\n"
"v_and_b32 v123, s4, v122\n"
"v_lshlrev_b32 v120, 16, v120\n"
"v_lshlrev_b32 v122, 16, v122\n"
"v_pk_mul_f32 v[120:121], v[120:121], v[222:223] op_sel:[0,1]\n"
"v_pk_mul_f32 v[122:123], v[122:123], v[222:223] op_sel:[0,1]\n"
"v_cvt_pk_bf16_f32 v220, v120, v121\n"
"v_cvt_pk_bf16_f32 v221, v122, v123\n"
"s_waitcnt lgkmcnt(0)\n"
"v_mfma_f32_16x16x16_bf16 v[136:139], v[212:213], v[56:57], 0\n"
"s_movk_i32 s6, 0xff\n"
"v_mfma_f32_16x16x16_bf16 v[140:143], v[212:213], v[66:67], 0\n"
"s_lshl_b32 s7, s72, 10\n"
"v_mfma_f32_16x16x16_bf16 v[136:139], v[214:215], v[58:59], v[136:139]\n"
"s_add_u32 s7, s7, s81\n"
"v_mfma_f32_16x16x16_bf16 v[140:143], v[214:215], v[68:69], v[140:143]\n"
"v_readlane_b32 s68, v222, 0\n"
"v_mfma_f32_16x16x16_bf16 v[136:139], v[216:217], v[60:61], v[136:139]\n"
"v_readlane_b32 s69, v222, 1\n"
"v_mfma_f32_16x16x16_bf16 v[140:143], v[216:217], v[70:71], v[140:143]\n"
"v_readlane_b32 s70, v223, 0\n"
"v_mfma_f32_16x16x16_bf16 v[136:139], v[218:219], v[62:63], v[136:139]\n"
"v_readlane_b32 s79, v223, 1\n"
"v_mfma_f32_16x16x16_bf16 v[140:143], v[218:219], v[72:73], v[140:143]\n"
"v_mfma_f32_16x16x16_bf16 v[136:139], v[220:221], v[64:65], v[136:139]\n"
"v_mfma_f32_16x16x16_bf16 v[140:143], v[220:221], v[74:75], v[140:143]\n"
"s_nop 6\n"
"v_bfi_b32 v184, s6, v54, v136\n"
"v_bfi_b32 v185, s6, v54, v137\n"
"v_bfi_b32 v186, s6, v54, v140\n"
"v_bfi_b32 v187, s6, v54, v141\n"
"buffer_store_dwordx4 v[184:187], v55, s[20:23], s7 offen sc1\n"
"s_mov_b32 s34, 2621440\n"
"s_mov_b32 s35, 0x20000\n"
"v_and_b32 v47, 15, v0\n"
"v_lshrrev_b32 v48, 4, v1\n"
"s_lshl_b32 s66, s63, 4\n"
"v_add_u32 v44, s66, v47\n"
"v_xor_b32 v45, 16, v44\n"
"v_lshlrev_b32 v49, 4, v48\n"
"s_lshl_b32 s60, s40, 6\n"
"s_lshl3_add_u32 s61, s62, 256\n"
"s_mov_b32 s65, 151552\n"
"s_movk_i32 s66, 336\n"
"v_mad_u32_u24 v30, v44, s66, v49\n"
"v_mad_u32_u24 v31, v45, s66, v49\n"
"s_add_u32 s66, s65, s61\n"
"v_add_u32 v43, s66, v30\n"
"s_add_u32 s66, s65, s60\n"
"v_add_u32 v30, s66, v30\n"
"v_add_u32 v31, s66, v31\n"
"s_movk_i32 s66, 1056\n"
"v_mad_u32_u24 v10, v44, s66, v49\n"
"v_mad_u32_u24 v12, v45, s66, v49\n"
"s_lshl_b32 s66, s63, 6\n"
"s_sub_u32 s60, 128, s66\n"
"s_addk_i32 s66, 128\n"
"v_add_u32 v11, s60, v10\n"
"v_add_u32 v13, s60, v12\n"
"v_add_u32 v10, s66, v10\n"
"v_add_u32 v12, s66, v12\n"
"v_add_u32 v14, 67584, v10\n"
"v_add_u32 v15, 67584, v11\n"
"v_add_u32 v16, 67584, v12\n"
"v_add_u32 v17, 67584, v13\n"
"s_mul_i32 s66, s40, 2560\n"
"s_add_u32 s66, s66, 135296\n"
"v_add_u32 v18, s66, v2\n"
"v_lshl_add_u32 v19, v1, 3, s66\n"
"s_mov_b32 s60, s43\n"
"s_lshl1_add_u32 s66, s41, s60\n"
"s_lshl4_add_u32 s66, s66, s40\n"
"s_mulk_i32 s66, 9216\n"
"s_add_u32 s51, s66, 9437184\n"
"s_mul_i32 s66, s46, 36864\n"
"s_add_u32 s52, s51, s66\n"
"s_lshl_b32 s61, s60, 11\n"
"s_add_u32 s61, s61, s62\n"
"s_lshl_b32 s61, s61, 7\n"
"s_add_u32 s82, s74, s61\n"
"s_addk_i32 s82, 10240\n"
"s_lshl4_add_u32 s60, s60, s63\n"
"s_lshl_b32 s60, s60, 14\n"
"s_add_u32 s66, s74, s60\n"
"s_lshl_b32 s60, s40, 11\n"
"s_add_u32 s60, s60, s66\n"
"v_add_u32 v7, s60, v2\n"
"v_xor_b32 v8, 0x4000, v7\n"
"s_lshl_b32 s60, s62, 10\n"
"s_add_u32 s60, s60, s66\n"
"v_mov_b32 v119, s47\n"
"s_waitcnt vmcnt(5)\n"
"s_mov_b64 exec, 1\n"
"buffer_store_dword v119, off, s[20:23], s83 sc1\n"
"s_mov_b64 exec, -1\n"
"buffer_load_dwordx4 v5, s[12:15], s75 offen offset:2112  lds\n"
"s_addk_i32 s60, 8192\n"
"v_add_u32 v9, s60, v2\n"
"s_mov_b32 s74, s53\n"
"s_waitcnt lgkmcnt(0)\n"
"s_mov_b32 s6, 0xbfb8aa3b\n"
"s_mov_b32 s7, 0xbfb8aa3b\n"
"v_and_b32 v208, 0x7fffffff, v208\n"
"v_and_b32 v210, 0x7fffffff, v210\n"
"v_mul_f32 v209, v209, v208\n"
"v_mul_f32 v211, v211, v210\n"
"v_lshlrev_b32 v120, 16, v20\n"
"v_lshlrev_b32 v124, 16, v21\n"
"buffer_load_dwordx4 v6, s[12:15], s75 offen offset:3168  lds\n"
"v_and_b32 v121, s4, v20\n"
"v_and_b32 v125, s4, v21\n"
"v_pk_fma_f32 v[120:121], v[120:121], v[208:209], v[208:209] op_sel:[0,0,1] op_sel_hi:[1,0,1] neg_lo:[0,0,1] neg_hi:[0,0,1]\n"
"v_pk_fma_f32 v[124:125], v[124:125], v[208:209], v[208:209] op_sel:[0,0,1] op_sel_hi:[1,0,1] neg_lo:[0,0,1] neg_hi:[0,0,1]\n"
"v_pk_mul_f32 v[122:123], v[120:121], s[6:7]\n"
"v_pk_mul_f32 v[126:127], v[124:125], s[6:7]\n"
"v_exp_f32 v122, v122\n"
"v_exp_f32 v126, v126\n"
"v_exp_f32 v123, v123\n"
"v_exp_f32 v127, v127\n"
"v_pk_add_f32 v[122:123], v[122:123], 1.0 op_sel_hi:[1,0]\n"
"v_pk_add_f32 v[126:127], v[126:127], 1.0 op_sel_hi:[1,0]\n"
"s_add_u32 m0, s54, 38016\n"
"s_addk_i32 s75, 0x4000\n"
"buffer_load_dwordx4 v3, s[12:15], s75 offen offset:0  lds\n"
"v_rcp_f32 v122, v122\n"
"v_rcp_f32 v126, v126\n"
"v_rcp_f32 v123, v123\n"
"v_rcp_f32 v127, v127\n"
"v_pk_mul_f32 v[122:123], v[122:123], v[120:121]\n"
"v_pk_mul_f32 v[126:127], v[126:127], v[124:125]\n"
"v_cvt_pk_bf16_f32 v32, v122, v123\n"
"v_cvt_pk_bf16_f32 v33, v126, v127\n"
"v_lshlrev_b32 v120, 16, v22\n"
"v_lshlrev_b32 v124, 16, v23\n"
"v_and_b32 v121, s4, v22\n"
"v_and_b32 v125, s4, v23\n"
"buffer_load_dwordx4 v4, s[12:15], s75 offen offset:1056  lds\n"
"v_pk_fma_f32 v[120:121], v[120:121], v[208:209], v[208:209] op_sel:[0,0,1] op_sel_hi:[1,0,1] neg_lo:[0,0,1] neg_hi:[0,0,1]\n"
"v_pk_fma_f32 v[124:125], v[124:125], v[208:209], v[208:209] op_sel:[0,0,1] op_sel_hi:[1,0,1] neg_lo:[0,0,1] neg_hi:[0,0,1]\n"
"v_pk_mul_f32 v[122:123], v[120:121], s[6:7]\n"
"v_pk_mul_f32 v[126:127], v[124:125], s[6:7]\n"
"v_exp_f32 v122, v122\n"
"v_exp_f32 v126, v126\n"
"v_exp_f32 v123, v123\n"
"v_exp_f32 v127, v127\n"
"v_pk_add_f32 v[122:123], v[122:123], 1.0 op_sel_hi:[1,0]\n"
"v_pk_add_f32 v[126:127], v[126:127], 1.0 op_sel_hi:[1,0]\n"
"v_rcp_f32 v122, v122\n"
"v_rcp_f32 v126, v126\n"
"buffer_load_dwordx4 v5, s[12:15], s75 offen offset:2112  lds\n"
"v_rcp_f32 v123, v123\n"
"v_rcp_f32 v127, v127\n"
"v_pk_mul_f32 v[122:123], v[122:123], v[120:121]\n"
"v_pk_mul_f32 v[126:127], v[126:127], v[124:125]\n"
"v_cvt_pk_bf16_f32 v34, v122, v123\n"
"v_cvt_pk_bf16_f32 v35, v126, v127\n"
"v_lshlrev_b32 v120, 16, v24\n"
"v_lshlrev_b32 v124, 16, v25\n"
"v_and_b32 v121, s4, v24\n"
"v_and_b32 v125, s4, v25\n"
"v_pk_fma_f32 v[120:121], v[120:121], v[210:211], v[210:211] op_sel:[0,0,1] op_sel_hi:[1,0,1] neg_lo:[0,0,1] neg_hi:[0,0,1]\n"
"v_pk_fma_f32 v[124:125], v[124:125], v[210:211], v[210:211] op_sel:[0,0,1] op_sel_hi:[1,0,1] neg_lo:[0,0,1] neg_hi:[0,0,1]\n"
"buffer_load_dwordx4 v6, s[12:15], s75 offen offset:3168  lds\n"
"v_pk_mul_f32 v[122:123], v[120:121], s[6:7]\n"
"v_pk_mul_f32 v[126:127], v[124:125], s[6:7]\n"
"v_exp_f32 v122, v122\n"
"v_exp_f32 v126, v126\n"
"v_exp_f32 v123, v123\n"
"v_exp_f32 v127, v127\n"
"v_pk_add_f32 v[122:123], v[122:123], 1.0 op_sel_hi:[1,0]\n"
"v_pk_add_f32 v[126:127], v[126:127], 1.0 op_sel_hi:[1,0]\n"
"v_rcp_f32 v122, v122\n"
"v_rcp_f32 v126, v126\n"
"v_rcp_f32 v123, v123\n"
"v_rcp_f32 v127, v127\n"
"s_bfe_u32 s75, s49, 0x40004\n"
"s_lshl_b32 s75, s75, 21\n"
"s_add_u32 s75, s75, s50\n"
"s_add_u32 m0, s54, 67584\n"
"s_nop 0\n"
"buffer_load_dwordx4 v3, s[16:19], s75 offen offset:0  lds\n"
"v_pk_mul_f32 v[122:123], v[122:123], v[120:121]\n"
"v_pk_mul_f32 v[126:127], v[126:127], v[124:125]\n"
"v_cvt_pk_bf16_f32 v36, v122, v123\n"
"v_cvt_pk_bf16_f32 v37, v126, v127\n"
"v_lshlrev_b32 v120, 16, v26\n"
"v_lshlrev_b32 v124, 16, v27\n"
"v_and_b32 v121, s4, v26\n"
"v_and_b32 v125, s4, v27\n"
"v_pk_fma_f32 v[120:121], v[120:121], v[210:211], v[210:211] op_sel:[0,0,1] op_sel_hi:[1,0,1] neg_lo:[0,0,1] neg_hi:[0,0,1]\n"
"v_pk_fma_f32 v[124:125], v[124:125], v[210:211], v[210:211] op_sel:[0,0,1] op_sel_hi:[1,0,1] neg_lo:[0,0,1] neg_hi:[0,0,1]\n"
"v_pk_mul_f32 v[122:123], v[120:121], s[6:7]\n"
"v_pk_mul_f32 v[126:127], v[124:125], s[6:7]\n"
"buffer_load_dwordx4 v4, s[16:19], s75 offen offset:1056  lds\n"
"v_exp_f32 v122, v122\n"
"v_exp_f32 v126, v126\n"
"v_exp_f32 v123, v123\n"
"v_exp_f32 v127, v127\n"
"v_pk_add_f32 v[122:123], v[122:123], 1.0 op_sel_hi:[1,0]\n"
"v_pk_add_f32 v[126:127], v[126:127], 1.0 op_sel_hi:[1,0]\n"
"v_rcp_f32 v122, v122\n"
"v_rcp_f32 v126, v126\n"
"v_rcp_f32 v123, v123\n"
"v_rcp_f32 v127, v127\n"
"v_pk_mul_f32 v[122:123], v[122:123], v[120:121]\n"
"v_pk_mul_f32 v[126:127], v[126:127], v[124:125]\n"
"buffer_load_dwordx4 v5, s[16:19], s75 offen offset:2112  lds\n"
"v_cvt_pk_bf16_f32 v38, v122, v123\n"
"v_cvt_pk_bf16_f32 v39, v126, v127\n"
"v_lshlrev_b32 v120, 16, v28\n"
"v_lshlrev_b32 v124, 16, v29\n"
"v_and_b32 v121, s4, v28\n"
"v_and_b32 v125, s4, v29\n"
"v_pk_fma_f32 v[120:121], v[120:121], v[208:209], v[208:209] op_sel:[0,0,1] op_sel_hi:[1,0,1] neg_lo:[0,0,1] neg_hi:[0,0,1]\n"
"v_pk_fma_f32 v[124:125], v[124:125], v[208:209], v[208:209] op_sel:[0,0,1] op_sel_hi:[1,0,1] neg_lo:[0,0,1] neg_hi:[0,0,1]\n"
"v_pk_mul_f32 v[122:123], v[120:121], s[6:7]\n"
"v_pk_mul_f32 v[126:127], v[124:125], s[6:7]\n"
"v_exp_f32 v122, v122\n"
"v_exp_f32 v126, v126\n"
"buffer_load_dwordx4 v6, s[16:19], s75 offen offset:3168  lds\n"
"v_exp_f32 v123, v123\n"
"v_exp_f32 v127, v127\n"
"v_pk_add_f32 v[122:123], v[122:123], 1.0 op_sel_hi:[1,0]\n"
"v_pk_add_f32 v[126:127], v[126:127], 1.0 op_sel_hi:[1,0]\n"
"v_rcp_f32 v122, v122\n"
"v_rcp_f32 v126, v126\n"
"v_rcp_f32 v123, v123\n"
"v_rcp_f32 v127, v127\n"
"v_pk_mul_f32 v[122:123], v[122:123], v[120:121]\n"
"v_pk_mul_f32 v[126:127], v[126:127], v[124:125]\n"
"v_cvt_pk_bf16_f32 v40, v122, v123\n"
"v_cvt_pk_bf16_f32 v41, v126, v127\n"
"s_add_u32 m0, s54, 71808\n"
"s_addk_i32 s75, 0x4000\n"
"buffer_load_dwordx4 v3, s[16:19], s75 offen offset:0  lds\n"
"v_lshlrev_b32 v44, 4, v0\n"
"v_add_u32 v44, 151552, v44\n"
"s_barrier\n"
"ds_write_b128 v18, v[32:35]\n"
"ds_write_b128 v18, v[36:39] offset:1024\n"
"ds_write_b64 v19, v[40:41] offset:2048\n"
"ds_write_b128 v44, a[236:239]\n"
"ds_write_b128 v44, a[240:243] offset:4096\n"
"ds_write_b128 v44, a[244:247] offset:8192\n"
"s_bfe_u32 s65, s55, 0x10004\n"
"s_xor_b32 s65, s65, s63\n"
"s_lshl_b32 s65, s65, 3\n"
"buffer_load_dwordx4 v4, s[16:19], s75 offen offset:1056  lds\n"
"s_lshr_b32 s65, 0xffffff, s65\n"
"s_lshr_b32 s66, s55, 5\n"
"s_cmp_eq_u32 s66, s78\n"
"s_cselect_b32 s6, s65, 0\n"
"s_bfe_u32 s65, s56, 0x10004\n"
"s_xor_b32 s65, s65, s63\n"
"s_lshl_b32 s65, s65, 3\n"
"s_lshr_b32 s65, 0xffffff, s65\n"
"s_lshr_b32 s66, s56, 5\n"
"s_cmp_eq_u32 s66, s78\n"
"s_cselect_b32 s7, s65, 0\n"
"v_lshrrev_b32 v44, 5, v1\n"
"s_sub_u32 s66, s56, s55\n"
"v_mul_i32_i24 v45, s66, v44\n"
"buffer_load_dwordx4 v5, s[16:19], s75 offen offset:2112  lds\n"
"v_add_u32 v45, s55, v45\n"
"v_mul_u32_u24 v44, 320, v44\n"
"v_bfe_u32 v46, v45, 4, 1\n"
"v_xor_b32 v46, s63, v46\n"
"v_and_b32 v45, 15, v45\n"
"v_lshlrev_b32 v46, 10, v46\n"
"v_lshl_add_u32 v46, v45, 4, v46\n"
"v_bfe_u32 v47, v0, 2, 2\n"
"v_and_b32 v48, 3, v0\n"
"v_lshl_add_u32 v46, v47, 8, v46\n"
"v_lshl_add_u32 v46, v48, 2, v46\n"
"v_bfe_u32 v47, v0, 1, 2\n"
"buffer_load_dwordx4 v6, s[16:19], s75 offen offset:3168  lds\n"
"v_and_b32 v48, 1, v0\n"
"v_lshlrev_b32 v45, 3, v45\n"
"v_lshl_add_u32 v45, v47, 7, v45\n"
"v_lshl_add_u32 v45, v48, 2, v45\n"
"v_add_u32 v45, 2048, v45\n"
"v_lshl_add_u32 v47, v47, 2, v48\n"
"s_lshl1_add_u32 s66, s62, 64\n"
"v_add_u32 v47, s66, v47\n"
"v_and_b32 v48, 16, v0\n"
"v_cmp_ne_u32 vcc, 0, v48\n"
"s_lshl_b32 s66, s40, 4\n"
"v_and_b32 v48, 15, v0\n"
"s_bfe_u32 s75, s49, 0x40008\n"
"s_lshl_b32 s75, s75, 21\n"
"s_add_u32 s75, s75, s50\n"
"s_add_u32 m0, s54, 101376\n"
"s_nop 0\n"
"buffer_load_dwordx4 v3, s[16:19], s75 offen offset:0  lds\n"
"v_add_u32 v48, s66, v48\n"
"v_cndmask_b32 v46, v46, v45, vcc\n"
"v_cndmask_b32 v47, v48, v47, vcc\n"
"s_mul_i32 s66, s40, 2560\n"
"s_add_u32 s66, s66, 135296\n"
"v_add_u32 v53, s66, v46\n"
"s_mul_i32 s66, s44, 80\n"
"s_add_u32 s66, s66, 114688\n"
"v_add3_u32 v47, v47, v44, s66\n"
"v_lshlrev_b32 v47, 3, v47\n"
"v_mov_b32 v48, 1073741824\n"
"v_cndmask_b32_e64 v52, v48, v47, s[6:7]\n"
"buffer_load_dwordx4 v4, s[16:19], s75 offen offset:1056  lds\n"
"v_lshrrev_b32 v118, 2, v1\n"
"v_lshlrev_b32 v118, 14, v118\n"
"v_lshrrev_b32 v45, 2, v1\n"
"v_and_b32 v46, 3, v0\n"
"v_mul_u32_u24 v45, 160, v45\n"
"v_lshl_add_u32 v45, v46, 3, v45\n"
"v_add_u32 v45, 151552, v45\n"
"s_lshl_b32 s65, s40, 5\n"
"v_add_u32 v46, s65, v45\n"
"s_mul_i32 s60, s72, 160\n"
"s_add_u32 s60, s60, s71\n"
"s_lshl_b32 s60, s60, 7\n"
"buffer_load_dwordx4 v5, s[16:19], s75 offen offset:2112  lds\n"
"s_add_u32 s60, s60, 589824\n"
"s_add_u32 s61, s60, 16384\n"
"s_cmp_eq_u32 s40, 0\n"
"s_cselect_b32 s61, s61, 1073741824\n"
"s_lshl_b32 s66, s40, 12\n"
"s_add_u32 s60, s60, s66\n"
"v_lshrrev_b32 v48, 4, v1\n"
"v_and_b32 v44, 1, v0\n"
"v_lshlrev_b32 v47, 3, v48\n"
"v_lshl_add_u32 v44, v44, 7, v47\n"
"v_add_u32 v44, 145664, v44\n"
"v_and_b32 v49, 0xf0, v2\n"
"v_lshl_add_u32 v48, v48, 11, v49\n"
"buffer_load_dwordx4 v6, s[16:19], s75 offen offset:3168  lds\n"
"s_add_u32 s66, s65, 145664\n"
"v_lshrrev_b32 v49, 3, v49\n"
"v_add_u32 v49, s66, v49\n"
"v_lshrrev_b32 v47, 1, v55\n"
"s_cmp_eq_u32 s40, 0\n"
"s_cselect_b32 s68, s68, s69\n"
"s_cselect_b32 s70, s70, s79\n"
"s_and_b32 s66, s57, s4\n"
"v_mov_b32 v88, v224\n"
"v_mov_b32 v90, 0xbfb8aa3b\n"
"v_mul_f32 v90, s66, v90\n"
"v_exp_f32 v90, v90\n"
"v_and_b32 v89, s4, v88\n"
"v_lshlrev_b32 v88, 16, v88\n"
"s_add_u32 m0, s54, 105600\n"
"s_addk_i32 s75, 0x4000\n"
"buffer_load_dwordx4 v3, s[16:19], s75 offen offset:0  lds\n"
"v_add_f32 v90, 1.0, v90\n"
"v_rcp_f32 v90, v90\n"
"v_subrev_f32 v88, s68, v88\n"
"v_subrev_f32 v89, s68, v89\n"
"v_cvt_pk_bf16_f32 v88, v88, v89\n"
"v_cvt_pk_bf16_f32 v90, v90, v90\n"
"v_and_b32 v89, s4, v88\n"
"v_lshlrev_b32 v88, 16, v88\n"
"v_readfirstlane_b32 s79, v90\n"
"v_mul_f32 v88, s70, v88\n"
"v_mul_f32 v89, s70, v89\n"
"v_cvt_pk_bf16_f32 v54, v88, v89\n"
"buffer_load_dwordx4 v4, s[16:19], s75 offen offset:1056  lds\n"
"s_and_b32 s79, s79, s4\n"
"s_lshl_b32 s48, s58, 12\n"
"s_add_u32 s48, s48, 589824\n"
"s_lshl_b32 s66, s58, 6\n"
"s_add_u32 s66, s66, 917504\n"
"s_cmp_lt_u32 s80, 27\n"
"s_cselect_b32 s69, 1073741824, s66\n"
"s_cselect_b32 s48, 1073741824, s48\n"
"buffer_load_dwordx4 v5, s[16:19], s75 offen offset:2112  lds\n"
"buffer_load_dwordx4 v6, s[16:19], s75 offen offset:3168  lds\n"
"buffer_load_dword v119, v118, s[20:23], s82 offen sc1\n"
"v_mov_b32 v50, v118\n"
"s_xor_b32 s83, s82, 0x40000\n"
"buffer_load_dword v51, v50, s[20:23], s83 offen sc1\n"
"buffer_load_dwordx4 v[176:179], v48, s[20:23], s81 offen offset:0 sc1\n"
"buffer_load_dwordx4 v[180:183], v48, s[20:23], s81 offen offset:1024 sc1\n"
"s_waitcnt vmcnt(3)\n"
"v_cmp_ne_u32 vcc, s47, v119\n"
"s_cbranch_vccnz Lretry4_%=\n"
"Lback4_%=:\n"
"buffer_load_dwordx4 v[192:195], v7, s[20:23], s74 offen offset:0 sc1\n"
"buffer_load_dwordx4 v[204:207], v8, s[20:23], s74 offen offset:0 sc1\n"
"buffer_load_dwordx4 v[196:199], v7, s[20:23], s74 offen offset:1024 sc1\n"
"buffer_load_dwordx4 v[208:211], v8, s[20:23], s74 offen offset:1024 sc1\n"
"buffer_load_dwordx4 v[200:203], v9, s[20:23], s74 offen offset:0 sc1\n"
"s_waitcnt lgkmcnt(0)\n"
"s_barrier\n"
"s_add_u32 s74, s74, 0x8000\n"
"s_bitset0_b32 s74, 18\n"
"buffer_load_dwordx4 v[212:215], v7, s[20:23], s74 offen offset:0 sc1\n"
"ds_read_b64_tr_b16 v[160:161], v46 offset:0\n"
"ds_read_b64_tr_b16 v[162:163], v46 offset:2560\n"
"ds_read_b64_tr_b16 v[164:165], v46 offset:5120\n"
"ds_read_b64_tr_b16 v[166:167], v46 offset:7680\n"
"ds_read_b64_tr_b16 v[168:169], v45 offset:128\n"
"ds_read_b64_tr_b16 v[170:171], v45 offset:2688\n"
"ds_read_b64_tr_b16 v[172:173], v45 offset:5248\n"
"ds_read_b64_tr_b16 v[174:175], v45 offset:7808\n"
"s_waitcnt vmcnt(6)\n"
"v_xor_b32 v46, s3, v176\n"
"buffer_load_dwordx4 v[224:227], v8, s[20:23], s74 offen offset:0 sc1\n"
"v_bitop3_b32 v46, v46, v177, s3 bitop3:0xf6\n"
"v_bitop3_b32 v46, v46, v178, s3 bitop3:0xf6\n"
"v_bitop3_b32 v46, v46, v179, s3 bitop3:0xf6\n"
"v_bitop3_b32 v46, v46, v180, s3 bitop3:0xf6\n"
"v_bitop3_b32 v46, v46, v181, s3 bitop3:0xf6\n"
"v_bitop3_b32 v46, v46, v182, s3 bitop3:0xf6\n"
"v_bitop3_b32 v46, v46, v183, s3 bitop3:0xf6\n"
"v_and_b32 v46, 0xff, v46\n"
"v_cmp_ne_u32 vcc, 0, v46\n"
"s_cbranch_vccnz Lretry5_%=\n"
"Lback5_%=:\n"
"buffer_load_dwordx4 v[216:219], v7, s[20:23], s74 offen offset:1024 sc1\n"
"v_and_b32 v176, 0xffffff00, v176\n"
"v_and_b32 v177, 0xffffff00, v177\n"
"v_and_b32 v178, 0xffffff00, v178\n"
"v_and_b32 v179, 0xffffff00, v179\n"
"v_and_b32 v180, 0xffffff00, v180\n"
"v_and_b32 v181, 0xffffff00, v181\n"
"v_and_b32 v182, 0xffffff00, v182\n"
"v_and_b32 v183, 0xffffff00, v183\n"
"v_add_f32 v176, v176, v180\n"
"buffer_load_dwordx4 v[228:231], v8, s[20:23], s74 offen offset:1024 sc1\n"
"v_add_f32 v177, v177, v181\n"
"v_add_f32 v178, v178, v182\n"
"v_add_f32 v179, v179, v183\n"
"v_mov_b32 v180, v176\n"
"v_mov_b32 v181, v177\n"
"v_mov_b32 v182, v178\n"
"v_mov_b32 v183, v179\n"
"v_permlane16_swap_b32 v180, v176\n"
"v_permlane16_swap_b32 v181, v177\n"
"buffer_load_dwordx4 v[220:223], v9, s[20:23], s74 offen offset:0 sc1\n"
"v_permlane16_swap_b32 v182, v178\n"
"v_permlane16_swap_b32 v183, v179\n"
"v_add_f32 v176, v176, v180\n"
"v_add_f32 v177, v177, v181\n"
"v_add_f32 v178, v178, v182\n"
"v_add_f32 v179, v179, v183\n"
"v_mov_b32 v180, v176\n"
"v_mov_b32 v181, v177\n"
"v_mov_b32 v182, v178\n"
"s_add_u32 s74, s74, 0x8000\n"
"s_bitset0_b32 s74, 18\n"
"buffer_load_dwordx4 v[232:235], v7, s[20:23], s74 offen offset:0 sc1\n"
"v_mov_b32 v183, v179\n"
"v_permlane32_swap_b32 v180, v176\n"
"v_permlane32_swap_b32 v181, v177\n"
"v_permlane32_swap_b32 v182, v178\n"
"v_permlane32_swap_b32 v183, v179\n"
"v_add_f32 v176, v176, v180\n"
"v_add_f32 v177, v177, v181\n"
"v_add_f32 v178, v178, v182\n"
"v_add_f32 v179, v179, v183\n"
"buffer_load_dwordx4 v[244:247], v8, s[20:23], s74 offen offset:0 sc1\n"
"v_cvt_pk_bf16_f32 v180, v178, v179\n"
"v_cvt_pk_bf16_f32 v176, v176, v177\n"
"v_and_b32 v177, s4, v176\n"
"v_lshlrev_b32 v176, 16, v176\n"
"v_mul_f32 v178, 0xbfb8aa3b, v176\n"
"v_mul_f32 v179, 0xbfb8aa3b, v177\n"
"v_exp_f32 v178, v178\n"
"v_exp_f32 v179, v179\n"
"v_add_f32 v178, 1.0, v178\n"
"buffer_load_dwordx4 v[236:239], v7, s[20:23], s74 offen offset:1024 sc1\n"
"v_add_f32 v179, 1.0, v179\n"
"v_rcp_f32 v178, v178\n"
"v_rcp_f32 v179, v179\n"
"v_mul_f32 v178, v178, v176\n"
"v_mul_f32 v179, v179, v177\n"
"v_cvt_pk_bf16_f32 v176, v178, v179\n"
"v_and_b32 v177, s4, v176\n"
"v_lshlrev_b32 v176, 16, v176\n"
"v_and_b32 v181, s4, v180\n"
"buffer_load_dwordx4 v[248:251], v8, s[20:23], s74 offen offset:1024 sc1\n"
"v_lshlrev_b32 v180, 16, v180\n"
"v_mul_f32 v176, v176, v180\n"
"v_mul_f32 v177, v177, v181\n"
"v_cvt_pk_bf16_f32 v176, v176, v177\n"
"v_add_u32 v48, 163840, v47\n"
"ds_write_b16 v49, v176\n"
"ds_write_b16_d16_hi v49, v176 offset:128\n"
"s_waitcnt lgkmcnt(0)\n"
"s_barrier\n"
"ds_read_b64 v[176:177], v44 offset:0\n"
"buffer_load_dwordx4 v[240:243], v9, s[20:23], s74 offen offset:0 sc1\n"
"ds_read_b64 v[178:179], v44 offset:32\n"
"ds_read_b64 v[180:181], v44 offset:64\n"
"ds_read_b64 v[182:183], v44 offset:96\n"
"s_waitcnt lgkmcnt(0)\n"
"v_mfma_f32_16x16x16_bf16 v[184:187], v[176:177], v[160:161], 0\n"
"v_mfma_f32_16x16x16_bf16 v[188:191], v[176:177], v[168:169], 0\n"
"v_mfma_f32_16x16x16_bf16 v[184:187], v[178:179], v[162:163], v[184:187]\n"
"v_mfma_f32_16x16x16_bf16 v[188:191], v[178:179], v[170:171], v[188:191]\n"
"v_mfma_f32_16x16x16_bf16 v[184:187], v[180:181], v[164:165], v[184:187]\n"
"v_mfma_f32_16x16x16_bf16 v[188:191], v[180:181], v[172:173], v[188:191]\n"
"v_mfma_f32_16x16x16_bf16 v[184:187], v[182:183], v[166:167], v[184:187]\n"
"v_mfma_f32_16x16x16_bf16 v[188:191], v[182:183], v[174:175], v[188:191]\n"
"s_nop 6\n"
"v_mov_b32 v186, v185\n"
"v_mov_b32 v185, s47\n"
"v_mov_b32 v187, s47\n"
"v_mov_b32 v190, v189\n"
"v_mov_b32 v189, s47\n"
"v_mov_b32 v191, s47\n"
"buffer_store_dwordx2 v[184:185], v47, s[20:23], s60 offen offset:0 sc1\n"
"buffer_store_dwordx2 v[186:187], v48, s[20:23], s60 offen offset:0 sc1\n"
"buffer_store_dwordx2 v[188:189], v47, s[20:23], s61 offen offset:0 sc1\n"
"buffer_store_dwordx2 v[190:191], v48, s[20:23], s61 offen offset:0 sc1\n"
"ds_read_b128 v[128:131], v10 offset:0\n"
"ds_read_b128 v[132:135], v11 offset:64\n"
"ds_read_b128 v[136:139], v12 offset:0\n"
"ds_read_b128 v[140:143], v13 offset:64\n"
"s_waitcnt vmcnt(14) lgkmcnt(0)\n"
"v_mfma_f32_16x16x32_bf16 v[56:59], v[192:195], v[128:131], 0\n"
"ds_read_b128 v[144:147], v14 offset:0\n"
"s_add_u32 s74, s74, 0x8000\n"
"s_bitset0_b32 s74, 18\n"
"v_mfma_f32_16x16x32_bf16 v[56:59], v[204:207], v[132:135], v[56:59]\n"
"ds_read_b128 v[148:151], v15 offset:64\n"
"buffer_load_dwordx4 v[252:255], v7, s[20:23], s74 offen offset:0 sc1\n"
"v_mfma_f32_16x16x32_bf16 v[60:63], v[208:211], v[132:135], 0\n"
"ds_read_b128 v[152:155], v16 offset:0\n"
"v_mfma_f32_16x16x32_bf16 v[60:63], v[196:199], v[128:131], v[60:63]\n"
"ds_read_b128 v[156:159], v17 offset:64\n"
"v_mfma_f32_16x16x32_bf16 v[72:75], v[200:203], v[128:131], 0\n"
"v_mfma_f32_16x16x32_bf16 v[76:79], v[200:203], v[136:139], 0\n"
"buffer_load_dwordx4 a[8:11], v8, s[20:23], s74 offen offset:0 sc1\n"
"v_mfma_f32_16x16x32_bf16 v[64:67], v[192:195], v[136:139], 0\n"
"v_mfma_f32_16x16x32_bf16 v[64:67], v[204:207], v[140:143], v[64:67]\n"
"v_mfma_f32_16x16x32_bf16 v[68:71], v[208:211], v[140:143], 0\n"
"v_mfma_f32_16x16x32_bf16 v[68:71], v[196:199], v[136:139], v[68:71]\n"
"buffer_load_dwordx4 a[0:3], v7, s[20:23], s74 offen offset:1024 sc1\n"
"s_waitcnt lgkmcnt(0)\n"
"v_mfma_f32_16x16x32_bf16 v[80:83], v[192:195], v[144:147], 0\n"
"ds_read_b128 v[128:131], v14 offset:33792\n"
"v_mfma_f32_16x16x32_bf16 v[80:83], v[204:207], v[148:151], v[80:83]\n"
"ds_read_b128 v[132:135], v15 offset:33856\n"
"v_mfma_f32_16x16x32_bf16 v[84:87], v[208:211], v[148:151], 0\n"
"ds_read_b128 v[136:139], v16 offset:33792\n"
"v_mfma_f32_16x16x32_bf16 v[84:87], v[196:199], v[144:147], v[84:87]\n"
"ds_read_b128 v[140:143], v17 offset:33856\n"
"buffer_load_dwordx4 a[12:15], v8, s[20:23], s74 offen offset:1024 sc1\n"
"v_mfma_f32_16x16x32_bf16 v[96:99], v[200:203], v[144:147], 0\n"
"v_mfma_f32_16x16x32_bf16 v[100:103], v[200:203], v[152:155], 0\n"
"v_mfma_f32_16x16x32_bf16 v[88:91], v[192:195], v[152:155], 0\n"
"v_mfma_f32_16x16x32_bf16 v[88:91], v[204:207], v[156:159], v[88:91]\n"
"buffer_load_dwordx4 a[4:7], v9, s[20:23], s74 offen offset:0 sc1\n"
"s_add_u32 s74, s74, 0x8000\n"
"s_bitset0_b32 s74, 18\n"
"v_mfma_f32_16x16x32_bf16 v[92:95], v[208:211], v[156:159], 0\n"
"v_mfma_f32_16x16x32_bf16 v[92:95], v[196:199], v[152:155], v[92:95]\n"
"s_waitcnt lgkmcnt(0)\n"
"v_mfma_f32_16x16x32_bf16 v[104:107], v[192:195], v[128:131], 0\n"
"ds_read_b128 v[144:147], v10 offset:128\n"
"v_mfma_f32_16x16x32_bf16 v[104:107], v[204:207], v[132:135], v[104:107]\n"
"ds_read_b128 v[148:151], v11 offset:192\n"
"buffer_load_dwordx4 a[16:19], v7, s[20:23], s74 offen offset:0 sc1\n"
"v_mfma_f32_16x16x32_bf16 v[108:111], v[208:211], v[132:135], 0\n"
"ds_read_b128 v[152:155], v12 offset:128\n"
"v_mfma_f32_16x16x32_bf16 v[108:111], v[196:199], v[128:131], v[108:111]\n"
"ds_read_b128 v[156:159], v13 offset:192\n"
"v_mfma_f32_16x16x32_bf16 v[120:123], v[200:203], v[128:131], 0\n"
"v_mfma_f32_16x16x32_bf16 v[124:127], v[200:203], v[136:139], 0\n"
"buffer_load_dwordx4 a[28:31], v8, s[20:23], s74 offen offset:0 sc1\n"
"v_mfma_f32_16x16x32_bf16 v[112:115], v[192:195], v[136:139], 0\n"
"v_mfma_f32_16x16x32_bf16 v[112:115], v[204:207], v[140:143], v[112:115]\n"
"v_mfma_f32_16x16x32_bf16 v[116:119], v[208:211], v[140:143], 0\n"
"v_mfma_f32_16x16x32_bf16 v[116:119], v[196:199], v[136:139], v[116:119]\n"
"buffer_load_dwordx4 a[20:23], v7, s[20:23], s74 offen offset:1024 sc1\n"
"s_waitcnt vmcnt(17) lgkmcnt(0)\n"
"v_mfma_f32_16x16x32_bf16 v[56:59], v[212:215], v[144:147], v[56:59]\n"
"ds_read_b128 v[128:131], v14 offset:128\n"
"v_mfma_f32_16x16x32_bf16 v[56:59], v[224:227], v[148:151], v[56:59]\n"
"ds_read_b128 v[132:135], v15 offset:192\n"
"v_mfma_f32_16x16x32_bf16 v[60:63], v[228:231], v[148:151], v[60:63]\n"
"ds_read_b128 v[136:139], v16 offset:128\n"
"v_mfma_f32_16x16x32_bf16 v[60:63], v[216:219], v[144:147], v[60:63]\n"
"ds_read_b128 v[140:143], v17 offset:192\n"
"buffer_load_dwordx4 a[32:35], v8, s[20:23], s74 offen offset:1024 sc1\n"
"v_mfma_f32_16x16x32_bf16 v[72:75], v[220:223], v[144:147], v[72:75]\n"
"v_mfma_f32_16x16x32_bf16 v[76:79], v[220:223], v[152:155], v[76:79]\n"
"v_mfma_f32_16x16x32_bf16 v[64:67], v[212:215], v[152:155], v[64:67]\n"
"v_mfma_f32_16x16x32_bf16 v[64:67], v[224:227], v[156:159], v[64:67]\n"
"buffer_load_dwordx4 a[24:27], v9, s[20:23], s74 offen offset:0 sc1\n"
"s_add_u32 s74, s74, 0x8000\n"
"s_bitset0_b32 s74, 18\n"
"v_mfma_f32_16x16x32_bf16 v[68:71], v[228:231], v[156:159], v[68:71]\n"
"v_mfma_f32_16x16x32_bf16 v[68:71], v[216:219], v[152:155], v[68:71]\n"
"s_waitcnt lgkmcnt(0)\n"
"v_mfma_f32_16x16x32_bf16 v[80:83], v[212:215], v[128:131], v[80:83]\n"
"ds_read_b128 v[144:147], v10 offset:256\n"
"v_mfma_f32_16x16x32_bf16 v[80:83], v[224:227], v[132:135], v[80:83]\n"
"ds_read_b128 v[148:151], v11 offset:320\n"
"buffer_load_dwordx4 a[36:39], v7, s[20:23], s74 offen offset:0 sc1\n"
"v_mfma_f32_16x16x32_bf16 v[84:87], v[228:231], v[132:135], v[84:87]\n"
"ds_read_b128 v[152:155], v12 offset:256\n"
"v_mfma_f32_16x16x32_bf16 v[84:87], v[216:219], v[128:131], v[84:87]\n"
"ds_read_b128 v[156:159], v13 offset:320\n"
"v_mfma_f32_16x16x32_bf16 v[96:99], v[220:223], v[128:131], v[96:99]\n"
"v_mfma_f32_16x16x32_bf16 v[100:103], v[220:223], v[136:139], v[100:103]\n"
"buffer_load_dwordx4 a[48:51], v8, s[20:23], s74 offen offset:0 sc1\n"
"v_mfma_f32_16x16x32_bf16 v[88:91], v[212:215], v[136:139], v[88:91]\n"
"v_mfma_f32_16x16x32_bf16 v[88:91], v[224:227], v[140:143], v[88:91]\n"
"v_mfma_f32_16x16x32_bf16 v[92:95], v[228:231], v[140:143], v[92:95]\n"
"v_mfma_f32_16x16x32_bf16 v[92:95], v[216:219], v[136:139], v[92:95]\n"
"buffer_load_dwordx4 a[40:43], v7, s[20:23], s74 offen offset:1024 sc1\n"
"s_waitcnt vmcnt(17) lgkmcnt(0)\n"
"v_mfma_f32_16x16x32_bf16 v[56:59], v[232:235], v[144:147], v[56:59]\n"
"ds_read_b128 v[128:131], v14 offset:256\n"
"v_mfma_f32_16x16x32_bf16 v[56:59], v[244:247], v[148:151], v[56:59]\n"
"ds_read_b128 v[132:135], v15 offset:320\n"
"v_mfma_f32_16x16x32_bf16 v[60:63], v[248:251], v[148:151], v[60:63]\n"
"ds_read_b128 v[136:139], v16 offset:256\n"
"v_mfma_f32_16x16x32_bf16 v[60:63], v[236:239], v[144:147], v[60:63]\n"
"ds_read_b128 v[140:143], v17 offset:320\n"
"buffer_load_dwordx4 a[52:55], v8, s[20:23], s74 offen offset:1024 sc1\n"
"v_mfma_f32_16x16x32_bf16 v[72:75], v[240:243], v[144:147], v[72:75]\n"
"v_mfma_f32_16x16x32_bf16 v[76:79], v[240:243], v[152:155], v[76:79]\n"
"v_mfma_f32_16x16x32_bf16 v[64:67], v[232:235], v[152:155], v[64:67]\n"
"v_mfma_f32_16x16x32_bf16 v[64:67], v[244:247], v[156:159], v[64:67]\n"
"buffer_load_dwordx4 a[44:47], v9, s[20:23], s74 offen offset:0 sc1\n"
"s_add_u32 s74, s74, 0x8000\n"
"s_bitset0_b32 s74, 18\n"
"v_mfma_f32_16x16x32_bf16 v[68:71], v[248:251], v[156:159], v[68:71]\n"
"v_mfma_f32_16x16x32_bf16 v[68:71], v[236:239], v[152:155], v[68:71]\n"
"s_waitcnt lgkmcnt(0)\n"
"v_mfma_f32_16x16x32_bf16 v[80:83], v[232:235], v[128:131], v[80:83]\n"
"ds_read_b128 v[144:147], v10 offset:384\n"
"v_mfma_f32_16x16x32_bf16 v[80:83], v[244:247], v[132:135], v[80:83]\n"
"ds_read_b128 v[148:151], v11 offset:448\n"
"buffer_load_dwordx4 a[56:59], v7, s[20:23], s74 offen offset:0 sc1\n"
"v_mfma_f32_16x16x32_bf16 v[84:87], v[248:251], v[132:135], v[84:87]\n"
"ds_read_b128 v[152:155], v12 offset:384\n"
"v_mfma_f32_16x16x32_bf16 v[84:87], v[236:239], v[128:131], v[84:87]\n"
"ds_read_b128 v[156:159], v13 offset:448\n"
"v_mfma_f32_16x16x32_bf16 v[96:99], v[240:243], v[128:131], v[96:99]\n"
"v_mfma_f32_16x16x32_bf16 v[100:103], v[240:243], v[136:139], v[100:103]\n"
"buffer_load_dwordx4 a[68:71], v8, s[20:23], s74 offen offset:0 sc1\n"
"v_mfma_f32_16x16x32_bf16 v[88:91], v[232:235], v[136:139], v[88:91]\n"
"v_mfma_f32_16x16x32_bf16 v[88:91], v[244:247], v[140:143], v[88:91]\n"
"v_mfma_f32_16x16x32_bf16 v[92:95], v[248:251], v[140:143], v[92:95]\n"
"v_mfma_f32_16x16x32_bf16 v[92:95], v[236:239], v[136:139], v[92:95]\n"
"buffer_load_dwordx4 a[60:63], v7, s[20:23], s74 offen offset:1024 sc1\n"
"s_waitcnt vmcnt(13) lgkmcnt(0)\n"
"v_mfma_f32_16x16x32_bf16 v[56:59], v[252:255], v[144:147], v[56:59]\n"
"ds_read_b128 v[128:131], v14 offset:384\n"
"v_mfma_f32_16x16x32_bf16 v[56:59], a[8:11], v[148:151], v[56:59]\n"
"ds_read_b128 v[132:135], v15 offset:448\n"
"v_mfma_f32_16x16x32_bf16 v[60:63], a[12:15], v[148:151], v[60:63]\n"
"ds_read_b128 v[136:139], v16 offset:384\n"
"v_mfma_f32_16x16x32_bf16 v[60:63], a[0:3], v[144:147], v[60:63]\n"
"ds_read_b128 v[140:143], v17 offset:448\n"
"buffer_load_dwordx4 a[72:75], v8, s[20:23], s74 offen offset:1024 sc1\n"
"v_mfma_f32_16x16x32_bf16 v[72:75], a[4:7], v[144:147], v[72:75]\n"
"v_mfma_f32_16x16x32_bf16 v[76:79], a[4:7], v[152:155], v[76:79]\n"
"v_mfma_f32_16x16x32_bf16 v[64:67], v[252:255], v[152:155], v[64:67]\n"
"v_mfma_f32_16x16x32_bf16 v[64:67], a[8:11], v[156:159], v[64:67]\n"
"buffer_load_dwordx4 a[64:67], v9, s[20:23], s74 offen offset:0 sc1\n"
"s_add_u32 s74, s74, 0x8000\n"
"s_bitset0_b32 s74, 18\n"
"v_mfma_f32_16x16x32_bf16 v[68:71], a[12:15], v[156:159], v[68:71]\n"
"v_mfma_f32_16x16x32_bf16 v[68:71], a[0:3], v[152:155], v[68:71]\n"
"s_waitcnt lgkmcnt(0)\n"
"v_mfma_f32_16x16x32_bf16 v[80:83], v[252:255], v[128:131], v[80:83]\n"
"ds_read_b128 v[144:147], v10 offset:512\n"
"v_mfma_f32_16x16x32_bf16 v[80:83], a[8:11], v[132:135], v[80:83]\n"
"ds_read_b128 v[148:151], v11 offset:576\n"
"buffer_load_dwordx4 a[76:79], v7, s[20:23], s74 offen offset:0 sc1\n"
"v_mfma_f32_16x16x32_bf16 v[84:87], a[12:15], v[132:135], v[84:87]\n"
"ds_read_b128 v[152:155], v12 offset:512\n"
"v_mfma_f32_16x16x32_bf16 v[84:87], a[0:3], v[128:131], v[84:87]\n"
"ds_read_b128 v[156:159], v13 offset:576\n"
"v_mfma_f32_16x16x32_bf16 v[96:99], a[4:7], v[128:131], v[96:99]\n"
"v_mfma_f32_16x16x32_bf16 v[100:103], a[4:7], v[136:139], v[100:103]\n"
"buffer_load_dwordx4 a[88:91], v8, s[20:23], s74 offen offset:0 sc1\n"
"v_mfma_f32_16x16x32_bf16 v[88:91], v[252:255], v[136:139], v[88:91]\n"
"v_mfma_f32_16x16x32_bf16 v[88:91], a[8:11], v[140:143], v[88:91]\n"
"v_mfma_f32_16x16x32_bf16 v[92:95], a[12:15], v[140:143], v[92:95]\n"
"v_mfma_f32_16x16x32_bf16 v[92:95], a[0:3], v[136:139], v[92:95]\n"
"buffer_load_dwordx4 a[80:83], v7, s[20:23], s74 offen offset:1024 sc1\n"
"s_waitcnt vmcnt(13) lgkmcnt(0)\n"
"v_mfma_f32_16x16x32_bf16 v[56:59], a[16:19], v[144:147], v[56:59]\n"
"ds_read_b128 v[128:131], v14 offset:512\n"
"v_mfma_f32_16x16x32_bf16 v[56:59], a[28:31], v[148:151], v[56:59]\n"
"ds_read_b128 v[132:135], v15 offset:576\n"
"v_mfma_f32_16x16x32_bf16 v[60:63], a[32:35], v[148:151], v[60:63]\n"
"ds_read_b128 v[136:139], v16 offset:512\n"
"v_mfma_f32_16x16x32_bf16 v[60:63], a[20:23], v[144:147], v[60:63]\n"
"ds_read_b128 v[140:143], v17 offset:576\n"
"buffer_load_dwordx4 a[92:95], v8, s[20:23], s74 offen offset:1024 sc1\n"
"v_mfma_f32_16x16x32_bf16 v[72:75], a[24:27], v[144:147], v[72:75]\n"
"v_mfma_f32_16x16x32_bf16 v[76:79], a[24:27], v[152:155], v[76:79]\n"
"v_mfma_f32_16x16x32_bf16 v[64:67], a[16:19], v[152:155], v[64:67]\n"
"v_mfma_f32_16x16x32_bf16 v[64:67], a[28:31], v[156:159], v[64:67]\n"
"buffer_load_dwordx4 a[84:87], v9, s[20:23], s74 offen offset:0 sc1\n"
"s_add_u32 s74, s74, 0x8000\n"
"s_bitset0_b32 s74, 18\n"
"v_mfma_f32_16x16x32_bf16 v[68:71], a[32:35], v[156:159], v[68:71]\n"
"v_cmp_ne_u32 vcc, s47, v51\n"
"s_cbranch_vccnz Lretry3_%=\n"
"Lback3_%=:\n"
"v_mfma_f32_16x16x32_bf16 v[68:71], a[20:23], v[152:155], v[68:71]\n"
"v_xor_b32 v7, 0x40000, v7\n"
"s_waitcnt lgkmcnt(0)\n"
"v_mfma_f32_16x16x32_bf16 v[80:83], a[16:19], v[128:131], v[80:83]\n"
"ds_read_b128 v[144:147], v10 offset:640\n"
"v_xor_b32 v8, 0x40000, v8\n"
"v_mfma_f32_16x16x32_bf16 v[80:83], a[28:31], v[132:135], v[80:83]\n"
"ds_read_b128 v[148:151], v11 offset:704\n"
"v_xor_b32 v9, 0x40000, v9\n"
"v_mfma_f32_16x16x32_bf16 v[84:87], a[32:35], v[132:135], v[84:87]\n"
"ds_read_b128 v[152:155], v12 offset:640\n"
"buffer_load_dwordx4 a[96:99], v7, s[20:23], s74 offen offset:0 sc1\n"
"v_mfma_f32_16x16x32_bf16 v[84:87], a[20:23], v[128:131], v[84:87]\n"
"ds_read_b128 v[156:159], v13 offset:704\n"
"v_mfma_f32_16x16x32_bf16 v[96:99], a[24:27], v[128:131], v[96:99]\n"
"v_mfma_f32_16x16x32_bf16 v[100:103], a[24:27], v[136:139], v[100:103]\n"
"v_mfma_f32_16x16x32_bf16 v[88:91], a[16:19], v[136:139], v[88:91]\n"
"buffer_load_dwordx4 a[108:111], v8, s[20:23], s74 offen offset:0 sc1\n"
"v_mfma_f32_16x16x32_bf16 v[88:91], a[28:31], v[140:143], v[88:91]\n"
"v_mfma_f32_16x16x32_bf16 v[92:95], a[32:35], v[140:143], v[92:95]\n"
"v_mfma_f32_16x16x32_bf16 v[92:95], a[20:23], v[136:139], v[92:95]\n"
"s_waitcnt vmcnt(12) lgkmcnt(0)\n"
"v_mfma_f32_16x16x32_bf16 v[56:59], a[36:39], v[144:147], v[56:59]\n"
"ds_read_b128 v[128:131], v14 offset:640\n"
"buffer_load_dwordx4 a[100:103], v7, s[20:23], s74 offen offset:1024 sc1\n"
"v_mfma_f32_16x16x32_bf16 v[56:59], a[48:51], v[148:151], v[56:59]\n"
"ds_read_b128 v[132:135], v15 offset:704\n"
"v_mfma_f32_16x16x32_bf16 v[60:63], a[52:55], v[148:151], v[60:63]\n"
"ds_read_b128 v[136:139], v16 offset:640\n"
"v_mfma_f32_16x16x32_bf16 v[60:63], a[40:43], v[144:147], v[60:63]\n"
"ds_read_b128 v[140:143], v17 offset:704\n"
"v_mfma_f32_16x16x32_bf16 v[72:75], a[44:47], v[144:147], v[72:75]\n"
"buffer_load_dwordx4 a[112:115], v8, s[20:23], s74 offen offset:1024 sc1\n"
"v_mfma_f32_16x16x32_bf16 v[76:79], a[44:47], v[152:155], v[76:79]\n"
"v_mfma_f32_16x16x32_bf16 v[64:67], a[36:39], v[152:155], v[64:67]\n"
"v_mfma_f32_16x16x32_bf16 v[64:67], a[48:51], v[156:159], v[64:67]\n"
"v_mfma_f32_16x16x32_bf16 v[68:71], a[52:55], v[156:159], v[68:71]\n"
"buffer_load_dwordx4 a[104:107], v9, s[20:23], s74 offen offset:0 sc1\n"
"s_add_u32 s74, s74, 0x8000\n"
"s_bitset0_b32 s74, 18\n"
"v_mfma_f32_16x16x32_bf16 v[68:71], a[40:43], v[152:155], v[68:71]\n"
"s_waitcnt lgkmcnt(0)\n"
"v_mfma_f32_16x16x32_bf16 v[80:83], a[36:39], v[128:131], v[80:83]\n"
"ds_read_b128 v[144:147], v10 offset:768\n"
"v_mfma_f32_16x16x32_bf16 v[80:83], a[48:51], v[132:135], v[80:83]\n"
"ds_read_b128 v[148:151], v11 offset:832\n"
"v_mfma_f32_16x16x32_bf16 v[84:87], a[52:55], v[132:135], v[84:87]\n"
"ds_read_b128 v[152:155], v12 offset:768\n"
"buffer_load_dwordx4 a[116:119], v7, s[20:23], s74 offen offset:0 sc1\n"
"v_mfma_f32_16x16x32_bf16 v[84:87], a[40:43], v[128:131], v[84:87]\n"
"ds_read_b128 v[156:159], v13 offset:832\n"
"v_mfma_f32_16x16x32_bf16 v[96:99], a[44:47], v[128:131], v[96:99]\n"
"v_mfma_f32_16x16x32_bf16 v[100:103], a[44:47], v[136:139], v[100:103]\n"
"v_mfma_f32_16x16x32_bf16 v[88:91], a[36:39], v[136:139], v[88:91]\n"
"buffer_load_dwordx4 a[128:131], v8, s[20:23], s74 offen offset:0 sc1\n"
"v_mfma_f32_16x16x32_bf16 v[88:91], a[48:51], v[140:143], v[88:91]\n"
"v_mfma_f32_16x16x32_bf16 v[92:95], a[52:55], v[140:143], v[92:95]\n"
"v_mfma_f32_16x16x32_bf16 v[92:95], a[40:43], v[136:139], v[92:95]\n"
"s_mov_b64 exec, s[36:37]\n"
"buffer_load_dwordx4 v[160:163], v2, s[20:23], s48 offen offset:0 sc1\n"
"s_mov_b64 exec, -1\n"
"s_waitcnt vmcnt(13) lgkmcnt(0)\n"
"v_mfma_f32_16x16x32_bf16 v[56:59], a[56:59], v[144:147], v[56:59]\n"
"ds_read_b128 v[128:131], v14 offset:768\n"
"buffer_load_dwordx4 a[120:123], v7, s[20:23], s74 offen offset:1024 sc1\n"
"v_mfma_f32_16x16x32_bf16 v[56:59], a[68:71], v[148:151], v[56:59]\n"
"ds_read_b128 v[132:135], v15 offset:832\n"
"v_mfma_f32_16x16x32_bf16 v[60:63], a[72:75], v[148:151], v[60:63]\n"
"ds_read_b128 v[136:139], v16 offset:768\n"
"v_mfma_f32_16x16x32_bf16 v[60:63], a[60:63], v[144:147], v[60:63]\n"
"ds_read_b128 v[140:143], v17 offset:832\n"
"v_mfma_f32_16x16x32_bf16 v[72:75], a[64:67], v[144:147], v[72:75]\n"
"buffer_load_dwordx4 a[132:135], v8, s[20:23], s74 offen offset:1024 sc1\n"
"v_mfma_f32_16x16x32_bf16 v[76:79], a[64:67], v[152:155], v[76:79]\n"
"s_mov_b64 exec, s[36:37]\n"
"buffer_load_dwordx4 v[164:167], v2, s[20:23], s48 offen offset:1024 sc1\n"
"s_mov_b64 exec, -1\n"
"v_mfma_f32_16x16x32_bf16 v[64:67], a[56:59], v[152:155], v[64:67]\n"
"v_mfma_f32_16x16x32_bf16 v[64:67], a[68:71], v[156:159], v[64:67]\n"
"v_mfma_f32_16x16x32_bf16 v[68:71], a[72:75], v[156:159], v[68:71]\n"
"buffer_load_dwordx4 a[124:127], v9, s[20:23], s74 offen offset:0 sc1\n"
"s_add_u32 s74, s74, 0x8000\n"
"s_bitset0_b32 s74, 18\n"
"v_mfma_f32_16x16x32_bf16 v[68:71], a[60:63], v[152:155], v[68:71]\n"
"s_waitcnt lgkmcnt(0)\n"
"v_mfma_f32_16x16x32_bf16 v[80:83], a[56:59], v[128:131], v[80:83]\n"
"ds_read_b128 v[144:147], v10 offset:896\n"
"v_mfma_f32_16x16x32_bf16 v[80:83], a[68:71], v[132:135], v[80:83]\n"
"ds_read_b128 v[148:151], v11 offset:960\n"
"s_mov_b64 exec, s[36:37]\n"
"buffer_load_dwordx4 v[168:171], v2, s[20:23], s48 offen offset:2048 sc1\n"
"s_mov_b64 exec, -1\n"
"v_mfma_f32_16x16x32_bf16 v[84:87], a[72:75], v[132:135], v[84:87]\n"
"ds_read_b128 v[152:155], v12 offset:896\n"
"buffer_load_dwordx4 a[136:139], v7, s[20:23], s74 offen offset:0 sc1\n"
"v_mfma_f32_16x16x32_bf16 v[84:87], a[60:63], v[128:131], v[84:87]\n"
"ds_read_b128 v[156:159], v13 offset:960\n"
"v_mfma_f32_16x16x32_bf16 v[96:99], a[64:67], v[128:131], v[96:99]\n"
"v_mfma_f32_16x16x32_bf16 v[100:103], a[64:67], v[136:139], v[100:103]\n"
"v_mfma_f32_16x16x32_bf16 v[88:91], a[56:59], v[136:139], v[88:91]\n"
"buffer_load_dwordx4 a[148:151], v8, s[20:23], s74 offen offset:0 sc1\n"
"v_mfma_f32_16x16x32_bf16 v[88:91], a[68:71], v[140:143], v[88:91]\n"
"s_mov_b64 exec, s[36:37]\n"
"buffer_load_dwordx4 v[172:175], v2, s[20:23], s48 offen offset:3072 sc1\n"
"s_mov_b64 exec, -1\n"
"v_mfma_f32_16x16x32_bf16 v[92:95], a[72:75], v[140:143], v[92:95]\n"
"v_mfma_f32_16x16x32_bf16 v[92:95], a[60:63], v[136:139], v[92:95]\n"
"s_waitcnt vmcnt(16) lgkmcnt(0)\n"
"v_mfma_f32_16x16x32_bf16 v[56:59], a[76:79], v[144:147], v[56:59]\n"
"ds_read_b128 v[128:131], v14 offset:896\n"
"buffer_load_dwordx4 a[140:143], v7, s[20:23], s74 offen offset:1024 sc1\n"
"v_mfma_f32_16x16x32_bf16 v[56:59], a[88:91], v[148:151], v[56:59]\n"
"ds_read_b128 v[132:135], v15 offset:960\n"
"v_mfma_f32_16x16x32_bf16 v[60:63], a[92:95], v[148:151], v[60:63]\n"
"ds_read_b128 v[136:139], v16 offset:896\n"
"v_mfma_f32_16x16x32_bf16 v[60:63], a[80:83], v[144:147], v[60:63]\n"
"ds_read_b128 v[140:143], v17 offset:960\n"
"v_mfma_f32_16x16x32_bf16 v[72:75], a[84:87], v[144:147], v[72:75]\n"
"buffer_load_dwordx4 a[152:155], v8, s[20:23], s74 offen offset:1024 sc1\n"
"v_mfma_f32_16x16x32_bf16 v[76:79], a[84:87], v[152:155], v[76:79]\n"
"v_mfma_f32_16x16x32_bf16 v[64:67], a[76:79], v[152:155], v[64:67]\n"
"v_mfma_f32_16x16x32_bf16 v[64:67], a[88:91], v[156:159], v[64:67]\n"
"v_mfma_f32_16x16x32_bf16 v[68:71], a[92:95], v[156:159], v[68:71]\n"
"buffer_load_dwordx4 a[144:147], v9, s[20:23], s74 offen offset:0 sc1\n"
"v_mfma_f32_16x16x32_bf16 v[68:71], a[80:83], v[152:155], v[68:71]\n"
"s_waitcnt lgkmcnt(0)\n"
"v_mfma_f32_16x16x32_bf16 v[80:83], a[76:79], v[128:131], v[80:83]\n"
"ds_read_b128 v[144:147], v10 offset:33792\n"
"v_mfma_f32_16x16x32_bf16 v[80:83], a[88:91], v[132:135], v[80:83]\n"
"ds_read_b128 v[148:151], v11 offset:33856\n"
"v_mfma_f32_16x16x32_bf16 v[84:87], a[92:95], v[132:135], v[84:87]\n"
"ds_read_b128 v[152:155], v12 offset:33792\n"
"v_mfma_f32_16x16x32_bf16 v[84:87], a[80:83], v[128:131], v[84:87]\n"
"ds_read_b128 v[156:159], v13 offset:33856\n"
"v_mfma_f32_16x16x32_bf16 v[96:99], a[84:87], v[128:131], v[96:99]\n"
"v_mfma_f32_16x16x32_bf16 v[100:103], a[84:87], v[136:139], v[100:103]\n"
"v_mfma_f32_16x16x32_bf16 v[88:91], a[76:79], v[136:139], v[88:91]\n"
"v_mfma_f32_16x16x32_bf16 v[88:91], a[88:91], v[140:143], v[88:91]\n"
"v_mfma_f32_16x16x32_bf16 v[92:95], a[92:95], v[140:143], v[92:95]\n"
"v_mfma_f32_16x16x32_bf16 v[92:95], a[80:83], v[136:139], v[92:95]\n"
"s_waitcnt vmcnt(14) lgkmcnt(0)\n"
"s_barrier\n"
"v_mfma_f32_16x16x32_bf16 v[56:59], a[96:99], v[144:147], v[56:59]\n"
"ds_read_b128 v[128:131], v14 offset:33920\n"
"v_mfma_f32_16x16x32_bf16 v[56:59], a[108:111], v[148:151], v[56:59]\n"
"ds_read_b128 v[132:135], v15 offset:33984\n"
"s_bfe_u32 s75, s49, 0x40004\n"
"s_lshl_b32 s75, s75, 21\n"
"v_mfma_f32_16x16x32_bf16 v[60:63], a[112:115], v[148:151], v[60:63]\n"
"ds_read_b128 v[136:139], v16 offset:33920\n"
"s_add_u32 s75, s75, s50\n"
"s_add_u32 m0, s54, 0\n"
"v_mfma_f32_16x16x32_bf16 v[60:63], a[100:103], v[144:147], v[60:63]\n"
"ds_read_b128 v[140:143], v17 offset:33984\n"
"buffer_load_dwordx4 v3, s[12:15], s75 offen offset:0  lds\n"
"v_mfma_f32_16x16x32_bf16 v[72:75], a[104:107], v[144:147], v[72:75]\n"
"v_mfma_f32_16x16x32_bf16 v[76:79], a[104:107], v[152:155], v[76:79]\n"
"v_mfma_f32_16x16x32_bf16 v[64:67], a[96:99], v[152:155], v[64:67]\n"
"v_mfma_f32_16x16x32_bf16 v[64:67], a[108:111], v[156:159], v[64:67]\n"
"buffer_load_dwordx4 v4, s[12:15], s75 offen offset:1056  lds\n"
"v_mfma_f32_16x16x32_bf16 v[68:71], a[112:115], v[156:159], v[68:71]\n"
"v_mfma_f32_16x16x32_bf16 v[68:71], a[100:103], v[152:155], v[68:71]\n"
"s_waitcnt lgkmcnt(0)\n"
"v_mfma_f32_16x16x32_bf16 v[104:107], v[212:215], v[128:131], v[104:107]\n"
"ds_read_b128 v[144:147], v10 offset:33920\n"
"v_mfma_f32_16x16x32_bf16 v[104:107], v[224:227], v[132:135], v[104:107]\n"
"ds_read_b128 v[148:151], v11 offset:33984\n"
"buffer_load_dwordx4 v5, s[12:15], s75 offen offset:2112  lds\n"
"v_mfma_f32_16x16x32_bf16 v[108:111], v[228:231], v[132:135], v[108:111]\n"
"ds_read_b128 v[152:155], v12 offset:33920\n"
"v_mfma_f32_16x16x32_bf16 v[108:111], v[216:219], v[128:131], v[108:111]\n"
"ds_read_b128 v[156:159], v13 offset:33984\n"
"v_mfma_f32_16x16x32_bf16 v[120:123], v[220:223], v[128:131], v[120:123]\n"
"v_mfma_f32_16x16x32_bf16 v[124:127], v[220:223], v[136:139], v[124:127]\n"
"buffer_load_dwordx4 v6, s[12:15], s75 offen offset:3168  lds\n"
"s_add_u32 m0, s54, 4224\n"
"s_addk_i32 s75, 0x4000\n"
"v_mfma_f32_16x16x32_bf16 v[112:115], v[212:215], v[136:139], v[112:115]\n"
"v_mfma_f32_16x16x32_bf16 v[112:115], v[224:227], v[140:143], v[112:115]\n"
"v_mfma_f32_16x16x32_bf16 v[116:119], v[228:231], v[140:143], v[116:119]\n"
"v_mfma_f32_16x16x32_bf16 v[116:119], v[216:219], v[136:139], v[116:119]\n"
"buffer_load_dwordx4 v3, s[12:15], s75 offen offset:0  lds\n"
"s_waitcnt vmcnt(12) lgkmcnt(0)\n"
"v_mfma_f32_16x16x32_bf16 v[56:59], a[116:119], v[144:147], v[56:59]\n"
"ds_read_b128 v[128:131], v14 offset:34048\n"
"v_mfma_f32_16x16x32_bf16 v[56:59], a[128:131], v[148:151], v[56:59]\n"
"ds_read_b128 v[132:135], v15 offset:34112\n"
"v_mfma_f32_16x16x32_bf16 v[60:63], a[132:135], v[148:151], v[60:63]\n"
"ds_read_b128 v[136:139], v16 offset:34048\n"
"v_mfma_f32_16x16x32_bf16 v[60:63], a[120:123], v[144:147], v[60:63]\n"
"ds_read_b128 v[140:143], v17 offset:34112\n"
"buffer_load_dwordx4 v4, s[12:15], s75 offen offset:1056  lds\n"
"v_mfma_f32_16x16x32_bf16 v[72:75], a[124:127], v[144:147], v[72:75]\n"
"v_mfma_f32_16x16x32_bf16 v[76:79], a[124:127], v[152:155], v[76:79]\n"
"v_mfma_f32_16x16x32_bf16 v[64:67], a[116:119], v[152:155], v[64:67]\n"
"v_mfma_f32_16x16x32_bf16 v[64:67], a[128:131], v[156:159], v[64:67]\n"
"buffer_load_dwordx4 v5, s[12:15], s75 offen offset:2112  lds\n"
"v_mfma_f32_16x16x32_bf16 v[68:71], a[132:135], v[156:159], v[68:71]\n"
"v_mfma_f32_16x16x32_bf16 v[68:71], a[120:123], v[152:155], v[68:71]\n"
"s_waitcnt lgkmcnt(0)\n"
"v_mfma_f32_16x16x32_bf16 v[104:107], v[232:235], v[128:131], v[104:107]\n"
"ds_read_b128 v[144:147], v10 offset:34048\n"
"v_mfma_f32_16x16x32_bf16 v[104:107], v[244:247], v[132:135], v[104:107]\n"
"ds_read_b128 v[148:151], v11 offset:34112\n"
"buffer_load_dwordx4 v6, s[12:15], s75 offen offset:3168  lds\n"
"v_mfma_f32_16x16x32_bf16 v[108:111], v[248:251], v[132:135], v[108:111]\n"
"ds_read_b128 v[152:155], v12 offset:34048\n"
"s_add_u32 s74, s74, 0x8000\n"
"s_bitset0_b32 s74, 18\n"
"v_mfma_f32_16x16x32_bf16 v[108:111], v[236:239], v[128:131], v[108:111]\n"
"ds_read_b128 v[156:159], v13 offset:34112\n"
"v_mfma_f32_16x16x32_bf16 v[120:123], v[240:243], v[128:131], v[120:123]\n"
"v_mfma_f32_16x16x32_bf16 v[124:127], v[240:243], v[136:139], v[124:127]\n"
"buffer_load_dwordx4 a[156:159], v7, s[20:23], s74 offen offset:0 sc1\n"
"v_mfma_f32_16x16x32_bf16 v[112:115], v[232:235], v[136:139], v[112:115]\n"
"v_mfma_f32_16x16x32_bf16 v[112:115], v[244:247], v[140:143], v[112:115]\n"
"v_mfma_f32_16x16x32_bf16 v[116:119], v[248:251], v[140:143], v[116:119]\n"
"s_waitcnt vmcnt(12)\n"
"v_xor_b32 v48, s47, v161\n"
"v_bitop3_b32 v48, v48, v163, s47 bitop3:0xf6\n"
"v_mfma_f32_16x16x32_bf16 v[116:119], v[236:239], v[136:139], v[116:119]\n"
"buffer_load_dwordx4 a[168:171], v8, s[20:23], s74 offen offset:0 sc1\n"
"s_waitcnt vmcnt(10) lgkmcnt(0)\n"
"v_mfma_f32_16x16x32_bf16 v[56:59], a[136:139], v[144:147], v[56:59]\n"
"ds_read_b128 v[128:131], v14 offset:34176\n"
"v_bitop3_b32 v48, v48, v165, s47 bitop3:0xf6\n"
"v_mfma_f32_16x16x32_bf16 v[56:59], a[148:151], v[148:151], v[56:59]\n"
"ds_read_b128 v[132:135], v15 offset:34240\n"
"v_bitop3_b32 v48, v48, v167, s47 bitop3:0xf6\n"
"v_mfma_f32_16x16x32_bf16 v[60:63], a[152:155], v[148:151], v[60:63]\n"
"ds_read_b128 v[136:139], v16 offset:34176\n"
"v_bitop3_b32 v48, v48, v169, s47 bitop3:0xf6\n"
"v_mfma_f32_16x16x32_bf16 v[60:63], a[140:143], v[144:147], v[60:63]\n"
"ds_read_b128 v[140:143], v17 offset:34240\n"
"buffer_load_dwordx4 a[160:163], v7, s[20:23], s74 offen offset:1024 sc1\n"
"v_mfma_f32_16x16x32_bf16 v[72:75], a[144:147], v[144:147], v[72:75]\n"
"v_bitop3_b32 v48, v48, v171, s47 bitop3:0xf6\n"
"v_bitop3_b32 v48, v48, v173, s47 bitop3:0xf6\n"
"v_mfma_f32_16x16x32_bf16 v[76:79], a[144:147], v[152:155], v[76:79]\n"
"v_bitop3_b32 v48, v48, v175, s47 bitop3:0xf6\n"
"v_cmp_ne_u32 vcc, 0, v48\n"
"s_nop 0\n"
"s_and_b64 vcc, vcc, s[38:39]\n"
"s_cbranch_vccnz Lretry6_%=\n"
"Lback6_%=:\n"
"v_mfma_f32_16x16x32_bf16 v[64:67], a[136:139], v[152:155], v[64:67]\n"
"v_add_f32 v160, v160, v164\n"
"v_add_f32 v162, v162, v166\n"
"v_mfma_f32_16x16x32_bf16 v[64:67], a[148:151], v[156:159], v[64:67]\n"
"buffer_load_dwordx4 a[172:175], v8, s[20:23], s74 offen offset:1024 sc1\n"
"v_add_f32 v160, v160, v168\n"
"v_mfma_f32_16x16x32_bf16 v[68:71], a[152:155], v[156:159], v[68:71]\n"
"v_add_f32 v162, v162, v170\n"
"v_add_f32 v160, v160, v172\n"
"v_mfma_f32_16x16x32_bf16 v[68:71], a[140:143], v[152:155], v[68:71]\n"
"v_add_f32 v162, v162, v174\n"
"s_waitcnt lgkmcnt(0)\n"
"v_mfma_f32_16x16x32_bf16 v[104:107], v[252:255], v[128:131], v[104:107]\n"
"ds_read_b128 v[144:147], v14 offset:34304\n"
"v_and_b32 v165, s4, v54\n"
"v_mfma_f32_16x16x32_bf16 v[104:107], a[8:11], v[132:135], v[104:107]\n"
"ds_read_b128 v[148:151], v15 offset:34368\n"
"buffer_load_dwordx4 a[164:167], v9, s[20:23], s74 offen offset:0 sc1\n"
"v_mfma_f32_16x16x32_bf16 v[108:111], a[12:15], v[132:135], v[108:111]\n"
"ds_read_b128 v[152:155], v16 offset:34304\n"
"s_add_u32 s74, s74, 0x8000\n"
"s_bitset0_b32 s74, 18\n"
"v_mfma_f32_16x16x32_bf16 v[108:111], a[0:3], v[128:131], v[108:111]\n"
"ds_read_b128 v[156:159], v17 offset:34368\n"
"v_lshlrev_b32 v164, 16, v54\n"
"v_mfma_f32_16x16x32_bf16 v[120:123], a[4:7], v[128:131], v[120:123]\n"
"v_add_f32_dpp v160, v160, v160 row_ror:8 row_mask:0xf bank_mask:0xf\n"
"v_add_f32_dpp v162, v162, v162 row_ror:8 row_mask:0xf bank_mask:0xf\n"
"v_mfma_f32_16x16x32_bf16 v[124:127], a[4:7], v[136:139], v[124:127]\n"
"buffer_load_dwordx4 a[176:179], v7, s[20:23], s74 offen offset:0 sc1\n"
"v_mov_b32 v161, v160\n"
"v_mfma_f32_16x16x32_bf16 v[112:115], v[252:255], v[136:139], v[112:115]\n"
"v_mov_b32 v163, v162\n"
"v_permlane16_swap_b32 v161, v160\n"
"v_mfma_f32_16x16x32_bf16 v[112:115], a[8:11], v[140:143], v[112:115]\n"
"v_permlane16_swap_b32 v163, v162\n"
"v_mfma_f32_16x16x32_bf16 v[116:119], a[12:15], v[140:143], v[116:119]\n"
"v_add_f32 v160, v160, v161\n"
"v_add_f32 v162, v162, v163\n"
"v_mfma_f32_16x16x32_bf16 v[116:119], a[0:3], v[136:139], v[116:119]\n"
"buffer_load_dwordx4 a[188:191], v8, s[20:23], s74 offen offset:0 sc1\n"
"s_waitcnt lgkmcnt(0)\n"
"v_mfma_f32_16x16x32_bf16 v[104:107], a[16:19], v[144:147], v[104:107]\n"
"ds_read_b128 v[128:131], v14 offset:34432\n"
"v_mov_b32 v161, v160\n"
"v_mfma_f32_16x16x32_bf16 v[104:107], a[28:31], v[148:151], v[104:107]\n"
"ds_read_b128 v[132:135], v15 offset:34496\n"
"v_mov_b32 v163, v162\n"
"v_mfma_f32_16x16x32_bf16 v[108:111], a[32:35], v[148:151], v[108:111]\n"
"ds_read_b128 v[136:139], v16 offset:34432\n"
"v_permlane32_swap_b32 v161, v160\n"
"v_mfma_f32_16x16x32_bf16 v[108:111], a[20:23], v[144:147], v[108:111]\n"
"ds_read_b128 v[140:143], v17 offset:34496\n"
"buffer_load_dwordx4 a[180:183], v7, s[20:23], s74 offen offset:1024 sc1\n"
"v_mfma_f32_16x16x32_bf16 v[120:123], a[24:27], v[144:147], v[120:123]\n"
"v_permlane32_swap_b32 v163, v162\n"
"v_mfma_f32_16x16x32_bf16 v[124:127], a[24:27], v[152:155], v[124:127]\n"
"v_add_f32 v160, v160, v161\n"
"v_add_f32 v162, v162, v163\n"
"v_mfma_f32_16x16x32_bf16 v[112:115], a[16:19], v[152:155], v[112:115]\n"
"v_cvt_pk_bf16_f32 v160, v160, v162\n"
"v_lshlrev_b32 v46, 3, v1\n"
"v_mfma_f32_16x16x32_bf16 v[112:115], a[28:31], v[156:159], v[112:115]\n"
"buffer_load_dwordx4 a[192:195], v8, s[20:23], s74 offen offset:1024 sc1\n"
"v_and_b32 v161, s4, v160\n"
"v_mfma_f32_16x16x32_bf16 v[116:119], a[32:35], v[156:159], v[116:119]\n"
"v_lshlrev_b32 v160, 16, v160\n"
"v_mul_f32 v160, s79, v160\n"
"v_mfma_f32_16x16x32_bf16 v[116:119], a[20:23], v[152:155], v[116:119]\n"
"v_mul_f32 v161, s79, v161\n"
"s_waitcnt lgkmcnt(0)\n"
"v_mfma_f32_16x16x32_bf16 v[104:107], a[36:39], v[128:131], v[104:107]\n"
"ds_read_b128 v[144:147], v14 offset:34560\n"
"v_cvt_pk_bf16_f32 v160, v160, v161\n"
"v_mfma_f32_16x16x32_bf16 v[104:107], a[48:51], v[132:135], v[104:107]\n"
"ds_read_b128 v[148:151], v15 offset:34624\n"
"buffer_load_dwordx4 a[184:187], v9, s[20:23], s74 offen offset:0 sc1\n"
"v_mfma_f32_16x16x32_bf16 v[108:111], a[52:55], v[132:135], v[108:111]\n"
"ds_read_b128 v[152:155], v16 offset:34560\n"
"s_add_u32 s74, s74, 0x8000\n"
"s_bitset0_b32 s74, 18\n"
"v_mfma_f32_16x16x32_bf16 v[108:111], a[40:43], v[128:131], v[108:111]\n"
"ds_read_b128 v[156:159], v17 offset:34624\n"
"v_and_b32 v161, s4, v160\n"
"v_mfma_f32_16x16x32_bf16 v[120:123], a[44:47], v[128:131], v[120:123]\n"
"v_lshlrev_b32 v160, 16, v160\n"
"v_add_f32 v160, v160, v164\n"
"v_mfma_f32_16x16x32_bf16 v[124:127], a[44:47], v[136:139], v[124:127]\n"
"buffer_load_dwordx4 a[196:199], v7, s[20:23], s74 offen offset:0 sc1\n"
"v_add_f32 v161, v161, v165\n"
"v_mfma_f32_16x16x32_bf16 v[112:115], a[36:39], v[136:139], v[112:115]\n"
"v_cvt_pk_bf16_f32 v160, v160, v161\n"
"v_and_b32 v161, s4, v160\n"
"v_mfma_f32_16x16x32_bf16 v[112:115], a[48:51], v[140:143], v[112:115]\n"
"v_lshlrev_b32 v160, 16, v160\n"
"v_mul_f32 v162, 0xbfb8aa3b, v160\n"
"v_mfma_f32_16x16x32_bf16 v[116:119], a[52:55], v[140:143], v[116:119]\n"
"v_mul_f32 v163, 0xbfb8aa3b, v161\n"
"v_exp_f32 v162, v162\n"
"v_mfma_f32_16x16x32_bf16 v[116:119], a[40:43], v[136:139], v[116:119]\n"
"buffer_load_dwordx4 a[208:211], v8, s[20:23], s74 offen offset:0 sc1\n"
"s_waitcnt lgkmcnt(0)\n"
"v_mfma_f32_16x16x32_bf16 v[104:107], a[56:59], v[144:147], v[104:107]\n"
"ds_read_b128 v[128:131], v14 offset:34688\n"
"v_exp_f32 v163, v163\n"
"v_mfma_f32_16x16x32_bf16 v[104:107], a[68:71], v[148:151], v[104:107]\n"
"ds_read_b128 v[132:135], v15 offset:34752\n"
"v_add_f32 v162, 1.0, v162\n"
"v_mfma_f32_16x16x32_bf16 v[108:111], a[72:75], v[148:151], v[108:111]\n"
"ds_read_b128 v[136:139], v16 offset:34688\n"
"v_add_f32 v163, 1.0, v163\n"
"v_mfma_f32_16x16x32_bf16 v[108:111], a[60:63], v[144:147], v[108:111]\n"
"ds_read_b128 v[140:143], v17 offset:34752\n"
"buffer_load_dwordx4 a[200:203], v7, s[20:23], s74 offen offset:1024 sc1\n"
"v_mfma_f32_16x16x32_bf16 v[120:123], a[64:67], v[144:147], v[120:123]\n"
"v_rcp_f32 v162, v162\n"
"v_mfma_f32_16x16x32_bf16 v[124:127], a[64:67], v[152:155], v[124:127]\n"
"v_rcp_f32 v163, v163\n"
"v_mfma_f32_16x16x32_bf16 v[112:115], a[56:59], v[152:155], v[112:115]\n"
"v_mul_f32 v162, v162, v160\n"
"v_mul_f32 v163, v163, v161\n"
"v_mfma_f32_16x16x32_bf16 v[112:115], a[68:71], v[156:159], v[112:115]\n"
"buffer_load_dwordx4 a[212:215], v8, s[20:23], s74 offen offset:1024 sc1\n"
"v_cvt_pk_bf16_f32 v160, v162, v163\n"
"v_mfma_f32_16x16x32_bf16 v[116:119], a[72:75], v[156:159], v[116:119]\n"
"v_mov_b32 v161, s47\n"
"s_mov_b64 exec, 0xff\n"
"buffer_store_dwordx2 v[160:161], v46, s[20:23], s69 offen sc1\n"
"s_mov_b64 exec, -1\n"
"v_mfma_f32_16x16x32_bf16 v[116:119], a[60:63], v[152:155], v[116:119]\n"
"s_waitcnt lgkmcnt(0)\n"
"v_mfma_f32_16x16x32_bf16 v[104:107], a[76:79], v[128:131], v[104:107]\n"
"ds_read_b128 v[144:147], v10 offset:34176\n"
"v_mfma_f32_16x16x32_bf16 v[104:107], a[88:91], v[132:135], v[104:107]\n"
"ds_read_b128 v[148:151], v11 offset:34240\n"
"buffer_load_dwordx4 a[204:207], v9, s[20:23], s74 offen offset:0 sc1\n"
"v_mfma_f32_16x16x32_bf16 v[108:111], a[92:95], v[132:135], v[108:111]\n"
"ds_read_b128 v[152:155], v12 offset:34176\n"
"s_add_u32 s74, s74, 0x8000\n"
"s_bitset0_b32 s74, 18\n"
"v_mfma_f32_16x16x32_bf16 v[108:111], a[80:83], v[128:131], v[108:111]\n"
"ds_read_b128 v[156:159], v13 offset:34240\n"
"v_mfma_f32_16x16x32_bf16 v[120:123], a[84:87], v[128:131], v[120:123]\n"
"v_mfma_f32_16x16x32_bf16 v[124:127], a[84:87], v[136:139], v[124:127]\n"
"buffer_load_dwordx4 a[216:219], v7, s[20:23], s74 offen offset:0 sc1\n"
"v_mfma_f32_16x16x32_bf16 v[112:115], a[76:79], v[136:139], v[112:115]\n"
"v_mfma_f32_16x16x32_bf16 v[112:115], a[88:91], v[140:143], v[112:115]\n"
"v_mfma_f32_16x16x32_bf16 v[116:119], a[92:95], v[140:143], v[116:119]\n"
"v_mfma_f32_16x16x32_bf16 v[116:119], a[80:83], v[136:139], v[116:119]\n"
"buffer_load_dwordx4 a[228:231], v8, s[20:23], s74 offen offset:0 sc1\n"
"s_waitcnt vmcnt(13) lgkmcnt(0)\n"
"s_barrier\n"
"v_mfma_f32_16x16x32_bf16 v[56:59], a[156:159], v[144:147], v[56:59]\n"
"ds_read_b128 v[128:131], v10 offset:0\n"
"v_mfma_f32_16x16x32_bf16 v[56:59], a[168:171], v[148:151], v[56:59]\n"
"ds_read_b128 v[132:135], v11 offset:64\n"
"v_mfma_f32_16x16x32_bf16 v[60:63], a[172:175], v[148:151], v[60:63]\n"
"ds_read_b128 v[136:139], v12 offset:0\n"
"v_mfma_f32_16x16x32_bf16 v[60:63], a[160:163], v[144:147], v[60:63]\n"
"ds_read_b128 v[140:143], v13 offset:64\n"
"buffer_load_dwordx4 a[220:223], v7, s[20:23], s74 offen offset:1024 sc1\n"
"v_mfma_f32_16x16x32_bf16 v[72:75], a[164:167], v[144:147], v[72:75]\n"
"v_mfma_f32_16x16x32_bf16 v[76:79], a[164:167], v[152:155], v[76:79]\n"
"v_mfma_f32_16x16x32_bf16 v[64:67], a[156:159], v[152:155], v[64:67]\n"
"v_mfma_f32_16x16x32_bf16 v[64:67], a[168:171], v[156:159], v[64:67]\n"
"buffer_load_dwordx4 a[232:235], v8, s[20:23], s74 offen offset:1024 sc1\n"
"v_mfma_f32_16x16x32_bf16 v[68:71], a[172:175], v[156:159], v[68:71]\n"
"v_mfma_f32_16x16x32_bf16 v[68:71], a[160:163], v[152:155], v[68:71]\n"
"s_waitcnt lgkmcnt(0)\n"
"v_mfma_f32_16x16x32_bf16 v[80:83], a[96:99], v[128:131], v[80:83]\n"
"ds_read_b128 v[144:147], v10 offset:34304\n"
"v_mfma_f32_16x16x32_bf16 v[80:83], a[108:111], v[132:135], v[80:83]\n"
"ds_read_b128 v[148:151], v11 offset:34368\n"
"buffer_load_dwordx4 a[224:227], v9, s[20:23], s74 offen offset:0 sc1\n"
"v_mfma_f32_16x16x32_bf16 v[84:87], a[112:115], v[132:135], v[84:87]\n"
"ds_read_b128 v[152:155], v12 offset:34304\n"
"s_add_u32 s74, s74, 0x8000\n"
"s_bitset0_b32 s74, 18\n"
"v_mfma_f32_16x16x32_bf16 v[84:87], a[100:103], v[128:131], v[84:87]\n"
"ds_read_b128 v[156:159], v13 offset:34368\n"
"v_mfma_f32_16x16x32_bf16 v[96:99], a[104:107], v[128:131], v[96:99]\n"
"v_mfma_f32_16x16x32_bf16 v[100:103], a[104:107], v[136:139], v[100:103]\n"
"buffer_load_dwordx4 a[236:239], v7, s[20:23], s74 offen offset:0 sc1\n"
"v_mfma_f32_16x16x32_bf16 v[88:91], a[96:99], v[136:139], v[88:91]\n"
"v_mfma_f32_16x16x32_bf16 v[88:91], a[108:111], v[140:143], v[88:91]\n"
"v_mfma_f32_16x16x32_bf16 v[92:95], a[112:115], v[140:143], v[92:95]\n"
"v_mfma_f32_16x16x32_bf16 v[92:95], a[100:103], v[136:139], v[92:95]\n"
"buffer_load_dwordx4 a[248:251], v8, s[20:23], s74 offen offset:0 sc1\n"
"s_waitcnt vmcnt(13) lgkmcnt(0)\n"
"v_mfma_f32_16x16x32_bf16 v[56:59], a[176:179], v[144:147], v[56:59]\n"
"ds_read_b128 v[128:131], v10 offset:128\n"
"v_mfma_f32_16x16x32_bf16 v[56:59], a[188:191], v[148:151], v[56:59]\n"
"ds_read_b128 v[132:135], v11 offset:192\n"
"v_mfma_f32_16x16x32_bf16 v[60:63], a[192:195], v[148:151], v[60:63]\n"
"ds_read_b128 v[136:139], v12 offset:128\n"
"v_mfma_f32_16x16x32_bf16 v[60:63], a[180:183], v[144:147], v[60:63]\n"
"ds_read_b128 v[140:143], v13 offset:192\n"
"buffer_load_dwordx4 a[240:243], v7, s[20:23], s74 offen offset:1024 sc1\n"
"v_mfma_f32_16x16x32_bf16 v[72:75], a[184:187], v[144:147], v[72:75]\n"
"v_mfma_f32_16x16x32_bf16 v[76:79], a[184:187], v[152:155], v[76:79]\n"
"v_mfma_f32_16x16x32_bf16 v[64:67], a[176:179], v[152:155], v[64:67]\n"
"v_mfma_f32_16x16x32_bf16 v[64:67], a[188:191], v[156:159], v[64:67]\n"
"buffer_load_dwordx4 a[252:255], v8, s[20:23], s74 offen offset:1024 sc1\n"
"v_mfma_f32_16x16x32_bf16 v[68:71], a[192:195], v[156:159], v[68:71]\n"
"v_mfma_f32_16x16x32_bf16 v[68:71], a[180:183], v[152:155], v[68:71]\n"
"s_waitcnt lgkmcnt(0)\n"
"v_mfma_f32_16x16x32_bf16 v[80:83], a[116:119], v[128:131], v[80:83]\n"
"ds_read_b128 v[144:147], v10 offset:34432\n"
"v_mfma_f32_16x16x32_bf16 v[80:83], a[128:131], v[132:135], v[80:83]\n"
"ds_read_b128 v[148:151], v11 offset:34496\n"
"buffer_load_dwordx4 a[244:247], v9, s[20:23], s74 offen offset:0 sc1\n"
"v_mfma_f32_16x16x32_bf16 v[84:87], a[132:135], v[132:135], v[84:87]\n"
"ds_read_b128 v[152:155], v12 offset:34432\n"
"v_mfma_f32_16x16x32_bf16 v[84:87], a[120:123], v[128:131], v[84:87]\n"
"ds_read_b128 v[156:159], v13 offset:34496\n"
"s_bfe_u32 s75, s49, 0x40008\n"
"s_lshl_b32 s75, s75, 21\n"
"v_mfma_f32_16x16x32_bf16 v[96:99], a[124:127], v[128:131], v[96:99]\n"
"s_add_u32 s75, s75, s50\n"
"s_add_u32 m0, s54, 67584\n"
"v_mfma_f32_16x16x32_bf16 v[100:103], a[124:127], v[136:139], v[100:103]\n"
"buffer_load_dwordx4 v3, s[12:15], s75 offen offset:0  lds\n"
"v_mfma_f32_16x16x32_bf16 v[88:91], a[116:119], v[136:139], v[88:91]\n"
"v_mfma_f32_16x16x32_bf16 v[88:91], a[128:131], v[140:143], v[88:91]\n"
"v_mfma_f32_16x16x32_bf16 v[92:95], a[132:135], v[140:143], v[92:95]\n"
"v_mfma_f32_16x16x32_bf16 v[92:95], a[120:123], v[136:139], v[92:95]\n"
"buffer_load_dwordx4 v4, s[12:15], s75 offen offset:1056  lds\n"
"s_waitcnt vmcnt(12) lgkmcnt(0)\n"
"v_mfma_f32_16x16x32_bf16 v[56:59], a[196:199], v[144:147], v[56:59]\n"
"ds_read_b128 v[128:131], v10 offset:256\n"
"v_mfma_f32_16x16x32_bf16 v[56:59], a[208:211], v[148:151], v[56:59]\n"
"ds_read_b128 v[132:135], v11 offset:320\n"
"v_mfma_f32_16x16x32_bf16 v[60:63], a[212:215], v[148:151], v[60:63]\n"
"ds_read_b128 v[136:139], v12 offset:256\n"
"v_mfma_f32_16x16x32_bf16 v[60:63], a[200:203], v[144:147], v[60:63]\n"
"ds_read_b128 v[140:143], v13 offset:320\n"
"buffer_load_dwordx4 v5, s[12:15], s75 offen offset:2112  lds\n"
"v_mfma_f32_16x16x32_bf16 v[72:75], a[204:207], v[144:147], v[72:75]\n"
"v_mfma_f32_16x16x32_bf16 v[76:79], a[204:207], v[152:155], v[76:79]\n"
"v_mfma_f32_16x16x32_bf16 v[64:67], a[196:199], v[152:155], v[64:67]\n"
"v_mfma_f32_16x16x32_bf16 v[64:67], a[208:211], v[156:159], v[64:67]\n"
"buffer_load_dwordx4 v6, s[12:15], s75 offen offset:3168  lds\n"
"s_add_u32 m0, s54, 71808\n"
"s_addk_i32 s75, 0x4000\n"
"v_mfma_f32_16x16x32_bf16 v[68:71], a[212:215], v[156:159], v[68:71]\n"
"v_mfma_f32_16x16x32_bf16 v[68:71], a[200:203], v[152:155], v[68:71]\n"
"s_waitcnt lgkmcnt(0)\n"
"v_mfma_f32_16x16x32_bf16 v[80:83], a[136:139], v[128:131], v[80:83]\n"
"ds_read_b128 v[144:147], v10 offset:34560\n"
"v_mfma_f32_16x16x32_bf16 v[80:83], a[148:151], v[132:135], v[80:83]\n"
"ds_read_b128 v[148:151], v11 offset:34624\n"
"buffer_load_dwordx4 v3, s[12:15], s75 offen offset:0  lds\n"
"v_mfma_f32_16x16x32_bf16 v[84:87], a[152:155], v[132:135], v[84:87]\n"
"ds_read_b128 v[152:155], v12 offset:34560\n"
"v_mfma_f32_16x16x32_bf16 v[84:87], a[140:143], v[128:131], v[84:87]\n"
"ds_read_b128 v[156:159], v13 offset:34624\n"
"v_mfma_f32_16x16x32_bf16 v[96:99], a[144:147], v[128:131], v[96:99]\n"
"v_mfma_f32_16x16x32_bf16 v[100:103], a[144:147], v[136:139], v[100:103]\n"
"buffer_load_dwordx4 v4, s[12:15], s75 offen offset:1056  lds\n"
"v_mfma_f32_16x16x32_bf16 v[88:91], a[136:139], v[136:139], v[88:91]\n"
"v_mfma_f32_16x16x32_bf16 v[88:91], a[148:151], v[140:143], v[88:91]\n"
"v_mfma_f32_16x16x32_bf16 v[92:95], a[152:155], v[140:143], v[92:95]\n"
"v_mfma_f32_16x16x32_bf16 v[92:95], a[140:143], v[136:139], v[92:95]\n"
"buffer_load_dwordx4 v5, s[12:15], s75 offen offset:2112  lds\n"
"s_waitcnt vmcnt(12) lgkmcnt(0)\n"
"v_mfma_f32_16x16x32_bf16 v[56:59], a[216:219], v[144:147], v[56:59]\n"
"ds_read_b128 v[128:131], v10 offset:384\n"
"v_mfma_f32_16x16x32_bf16 v[56:59], a[228:231], v[148:151], v[56:59]\n"
"ds_read_b128 v[132:135], v11 offset:448\n"
"v_mfma_f32_16x16x32_bf16 v[60:63], a[232:235], v[148:151], v[60:63]\n"
"ds_read_b128 v[136:139], v12 offset:384\n"
"v_mfma_f32_16x16x32_bf16 v[60:63], a[220:223], v[144:147], v[60:63]\n"
"ds_read_b128 v[140:143], v13 offset:448\n"
"buffer_load_dwordx4 v6, s[12:15], s75 offen offset:3168  lds\n"
"v_mfma_f32_16x16x32_bf16 v[72:75], a[224:227], v[144:147], v[72:75]\n"
"s_bfe_u32 s75, s49, 0x4000c\n"
"s_lshl_b32 s75, s75, 21\n"
"s_add_u32 s75, s75, s50\n"
"s_add_u32 m0, s54, 101376\n"
"v_mfma_f32_16x16x32_bf16 v[76:79], a[224:227], v[152:155], v[76:79]\n"
"v_mfma_f32_16x16x32_bf16 v[64:67], a[216:219], v[152:155], v[64:67]\n"
"v_mfma_f32_16x16x32_bf16 v[64:67], a[228:231], v[156:159], v[64:67]\n"
"v_mfma_f32_16x16x32_bf16 v[68:71], a[232:235], v[156:159], v[68:71]\n"
"v_mfma_f32_16x16x32_bf16 v[68:71], a[220:223], v[152:155], v[68:71]\n"
"buffer_load_dwordx4 v3, s[16:19], s75 offen offset:0  lds\n"
"s_waitcnt lgkmcnt(0)\n"
"v_mfma_f32_16x16x32_bf16 v[80:83], a[156:159], v[128:131], v[80:83]\n"
"ds_read_b128 v[144:147], v10 offset:34688\n"
"v_mfma_f32_16x16x32_bf16 v[80:83], a[168:171], v[132:135], v[80:83]\n"
"ds_read_b128 v[148:151], v11 offset:34752\n"
"v_mfma_f32_16x16x32_bf16 v[84:87], a[172:175], v[132:135], v[84:87]\n"
"ds_read_b128 v[152:155], v12 offset:34688\n"
"v_mfma_f32_16x16x32_bf16 v[84:87], a[160:163], v[128:131], v[84:87]\n"
"ds_read_b128 v[156:159], v13 offset:34752\n"
"v_mfma_f32_16x16x32_bf16 v[96:99], a[164:167], v[128:131], v[96:99]\n"
"v_mfma_f32_16x16x32_bf16 v[100:103], a[164:167], v[136:139], v[100:103]\n"
"buffer_load_dwordx4 v4, s[16:19], s75 offen offset:1056  lds\n"
"v_mfma_f32_16x16x32_bf16 v[88:91], a[156:159], v[136:139], v[88:91]\n"
"v_mfma_f32_16x16x32_bf16 v[88:91], a[168:171], v[140:143], v[88:91]\n"
"v_mfma_f32_16x16x32_bf16 v[92:95], a[172:175], v[140:143], v[92:95]\n"
"v_mfma_f32_16x16x32_bf16 v[92:95], a[160:163], v[136:139], v[92:95]\n"
"s_waitcnt vmcnt(10) lgkmcnt(0)\n"
"v_mfma_f32_16x16x32_bf16 v[56:59], a[236:239], v[144:147], v[56:59]\n"
"ds_read_b128 v[128:131], v10 offset:512\n"
"v_mfma_f32_16x16x32_bf16 v[56:59], a[248:251], v[148:151], v[56:59]\n"
"ds_read_b128 v[132:135], v11 offset:576\n"
"buffer_load_dwordx4 v5, s[16:19], s75 offen offset:2112  lds\n"
"v_mfma_f32_16x16x32_bf16 v[60:63], a[252:255], v[148:151], v[60:63]\n"
"ds_read_b128 v[136:139], v12 offset:512\n"
"v_mfma_f32_16x16x32_bf16 v[60:63], a[240:243], v[144:147], v[60:63]\n"
"ds_read_b128 v[140:143], v13 offset:576\n"
"s_lshl_b32 s77, s40, 10\n"
"s_add_u32 s77, s77, 147456\n"
"v_mfma_f32_16x16x32_bf16 v[72:75], a[244:247], v[144:147], v[72:75]\n"
"v_add_u32 v7, s77, v2\n"
"v_xor_b32 v8, 1024, v7\n"
"v_mfma_f32_16x16x32_bf16 v[76:79], a[244:247], v[152:155], v[76:79]\n"
"v_mul_u32_u24 v9, 12, v1\n"
"v_mfma_f32_16x16x32_bf16 v[64:67], a[236:239], v[152:155], v[64:67]\n"
"v_mfma_f32_16x16x32_bf16 v[64:67], a[248:251], v[156:159], v[64:67]\n"
"buffer_load_dwordx4 v6, s[16:19], s75 offen offset:3168  lds\n"
"s_add_u32 m0, s54, 105600\n"
"s_addk_i32 s75, 0x4000\n"
"v_mfma_f32_16x16x32_bf16 v[68:71], a[252:255], v[156:159], v[68:71]\n"
"v_mfma_f32_16x16x32_bf16 v[68:71], a[240:243], v[152:155], v[68:71]\n"
"s_waitcnt lgkmcnt(0)\n"
"v_mfma_f32_16x16x32_bf16 v[80:83], a[176:179], v[128:131], v[80:83]\n"
"ds_read_b128 v[144:147], v10 offset:640\n"
"v_mfma_f32_16x16x32_bf16 v[80:83], a[188:191], v[132:135], v[80:83]\n"
"ds_read_b128 v[148:151], v11 offset:704\n"
"v_mfma_f32_16x16x32_bf16 v[84:87], a[192:195], v[132:135], v[84:87]\n"
"ds_read_b128 v[152:155], v12 offset:640\n"
"v_mfma_f32_16x16x32_bf16 v[84:87], a[180:183], v[128:131], v[84:87]\n"
"ds_read_b128 v[156:159], v13 offset:704\n"
"buffer_load_dwordx4 v3, s[16:19], s75 offen offset:0  lds\n"
"v_mfma_f32_16x16x32_bf16 v[96:99], a[184:187], v[128:131], v[96:99]\n"
"v_mfma_f32_16x16x32_bf16 v[100:103], a[184:187], v[136:139], v[100:103]\n"
"v_mfma_f32_16x16x32_bf16 v[88:91], a[176:179], v[136:139], v[88:91]\n"
"ds_write_b128 v7, v[76:79] offset:0\n"
"v_mfma_f32_16x16x32_bf16 v[88:91], a[188:191], v[140:143], v[88:91]\n"
"s_bfe_u32 s77, s49, 0x40000\n"
"s_mul_i32 s77, s77, 36864\n"
"s_add_u32 s77, s77, s51\n"
"v_cvt_pk_bf16_f32 v32, v56, v57\n"
"v_mfma_f32_16x16x32_bf16 v[92:95], a[192:195], v[140:143], v[92:95]\n"
"v_cvt_pk_bf16_f32 v33, v58, v59\n"
"v_cvt_pk_bf16_f32 v34, v60, v61\n"
"v_mfma_f32_16x16x32_bf16 v[92:95], a[180:183], v[136:139], v[92:95]\n"
"buffer_load_dwordx4 v4, s[16:19], s75 offen offset:1056  lds\n"
"s_waitcnt lgkmcnt(1)\n"
"v_mfma_f32_16x16x32_bf16 v[80:83], a[196:199], v[144:147], v[80:83]\n"
"ds_read_b128 v[128:131], v10 offset:768\n"
"v_cvt_pk_bf16_f32 v35, v62, v63\n"
"v_mfma_f32_16x16x32_bf16 v[80:83], a[208:211], v[148:151], v[80:83]\n"
"ds_read_b128 v[132:135], v11 offset:832\n"
"buffer_store_dwordx4 v[32:35], v2, s[20:23], s77 offen offset:0 sc1\n"
"v_mfma_f32_16x16x32_bf16 v[84:87], a[212:215], v[148:151], v[84:87]\n"
"ds_read_b128 v[136:139], v12 offset:768\n"
"v_mfma_f32_16x16x32_bf16 v[84:87], a[200:203], v[144:147], v[84:87]\n"
"ds_read_b128 v[140:143], v13 offset:832\n"
"v_cvt_pk_bf16_f32 v36, v64, v65\n"
"v_mfma_f32_16x16x32_bf16 v[96:99], a[204:207], v[144:147], v[96:99]\n"
"v_cvt_pk_bf16_f32 v37, v66, v67\n"
"v_cvt_pk_bf16_f32 v38, v68, v69\n"
"v_mfma_f32_16x16x32_bf16 v[100:103], a[204:207], v[152:155], v[100:103]\n"
"buffer_load_dwordx4 v5, s[16:19], s75 offen offset:2112  lds\n"
"v_cvt_pk_bf16_f32 v39, v70, v71\n"
"v_mfma_f32_16x16x32_bf16 v[88:91], a[196:199], v[152:155], v[88:91]\n"
"v_mfma_f32_16x16x32_bf16 v[88:91], a[208:211], v[156:159], v[88:91]\n"
"s_waitcnt lgkmcnt(4)\n"
"s_barrier\n"
"ds_read_b128 v[76:79], v8 offset:0\n"
"v_mfma_f32_16x16x32_bf16 v[92:95], a[212:215], v[156:159], v[92:95]\n"
"v_mfma_f32_16x16x32_bf16 v[92:95], a[200:203], v[152:155], v[92:95]\n"
"s_waitcnt lgkmcnt(1)\n"
"v_mfma_f32_16x16x32_bf16 v[80:83], a[216:219], v[128:131], v[80:83]\n"
"ds_read_b128 v[144:147], v10 offset:896\n"
"v_mfma_f32_16x16x32_bf16 v[80:83], a[228:231], v[132:135], v[80:83]\n"
"ds_read_b128 v[148:151], v11 offset:960\n"
"buffer_load_dwordx4 v6, s[16:19], s75 offen offset:3168  lds\n"
"v_mfma_f32_16x16x32_bf16 v[84:87], a[232:235], v[132:135], v[84:87]\n"
"ds_read_b128 v[152:155], v12 offset:896\n"
"v_mfma_f32_16x16x32_bf16 v[84:87], a[220:223], v[128:131], v[84:87]\n"
"ds_read_b128 v[156:159], v13 offset:960\n"
"s_bfe_u32 s75, s49, 0x4000c\n"
"s_lshl_b32 s75, s75, 21\n"
"v_mfma_f32_16x16x32_bf16 v[96:99], a[224:227], v[128:131], v[96:99]\n"
"s_add_u32 s75, s75, s50\n"
"s_add_u32 m0, s54, 33792\n"
"v_mfma_f32_16x16x32_bf16 v[100:103], a[224:227], v[136:139], v[100:103]\n"
"buffer_store_dwordx4 v[36:39], v2, s[20:23], s77 offen offset:1024 sc1\n"
"v_mfma_f32_16x16x32_bf16 v[88:91], a[216:219], v[136:139], v[88:91]\n"
"v_mfma_f32_16x16x32_bf16 v[88:91], a[228:231], v[140:143], v[88:91]\n"
"buffer_load_dwordx4 v3, s[12:15], s75 offen offset:0  lds\n"
"s_waitcnt lgkmcnt(4)\n"
"v_add_f32 v72, v72, v76\n"
"v_mfma_f32_16x16x32_bf16 v[92:95], a[232:235], v[140:143], v[92:95]\n"
"v_add_f32 v73, v73, v77\n"
"v_add_f32 v74, v74, v78\n"
"v_mfma_f32_16x16x32_bf16 v[92:95], a[220:223], v[136:139], v[92:95]\n"
"v_add_f32 v75, v75, v79\n"
"s_waitcnt lgkmcnt(0)\n"
"s_waitcnt vmcnt(11)\n"
"s_barrier\n"
"v_mfma_f32_16x16x32_bf16 v[80:83], a[236:239], v[144:147], v[80:83]\n"
"ds_read_b128 v[128:131], v14 offset:0\n"
"v_cvt_pk_bf16_f32 v40, v72, v73\n"
"v_mfma_f32_16x16x32_bf16 v[80:83], a[248:251], v[148:151], v[80:83]\n"
"ds_read_b128 v[132:135], v15 offset:64\n"
"v_cvt_pk_bf16_f32 v41, v74, v75\n"
"v_mfma_f32_16x16x32_bf16 v[84:87], a[252:255], v[148:151], v[84:87]\n"
"ds_read_b128 v[136:139], v16 offset:0\n"
"v_add3_u32 v42, v32, v33, v34\n"
"v_mfma_f32_16x16x32_bf16 v[84:87], a[240:243], v[144:147], v[84:87]\n"
"ds_read_b128 v[140:143], v17 offset:64\n"
"s_or_b64 exec, s[6:7], 1\n"
"buffer_load_dwordx2 v[46:47], v52, s[20:23], 0 offen sc1\n"
"s_mov_b64 exec, -1\n"
"v_mfma_f32_16x16x32_bf16 v[96:99], a[244:247], v[144:147], v[96:99]\n"
"buffer_load_dwordx4 v4, s[12:15], s75 offen offset:1056  lds\n"
"v_add3_u32 v42, v42, v35, v36\n"
"v_mfma_f32_16x16x32_bf16 v[100:103], a[244:247], v[152:155], v[100:103]\n"
"v_add3_u32 v42, v42, v37, v38\n"
"v_add3_u32 v42, v42, v39, v40\n"
"v_mfma_f32_16x16x32_bf16 v[88:91], a[236:239], v[152:155], v[88:91]\n"
"v_add3_u32 v42, v42, v41, s47\n"
"v_mfma_f32_16x16x32_bf16 v[88:91], a[248:251], v[156:159], v[88:91]\n"
"v_mfma_f32_16x16x32_bf16 v[92:95], a[252:255], v[156:159], v[92:95]\n"
"buffer_store_dwordx3 v[40:42], v9, s[20:23], s77 offen offset:2048 sc1\n"
"v_mfma_f32_16x16x32_bf16 v[92:95], a[240:243], v[152:155], v[92:95]\n"
"s_waitcnt lgkmcnt(0)\n"
"v_mfma_f32_16x16x32_bf16 v[104:107], a[96:99], v[128:131], v[104:107]\n"
"ds_read_b128 v[144:147], v14 offset:128\n"
"buffer_load_dwordx4 v5, s[12:15], s75 offen offset:2112  lds\n"
"v_mfma_f32_16x16x32_bf16 v[104:107], a[108:111], v[132:135], v[104:107]\n"
"ds_read_b128 v[148:151], v15 offset:192\n"
"v_mfma_f32_16x16x32_bf16 v[108:111], a[112:115], v[132:135], v[108:111]\n"
"ds_read_b128 v[152:155], v16 offset:128\n"
"v_mfma_f32_16x16x32_bf16 v[108:111], a[100:103], v[128:131], v[108:111]\n"
"ds_read_b128 v[156:159], v17 offset:192\n"
"v_mfma_f32_16x16x32_bf16 v[120:123], a[104:107], v[128:131], v[120:123]\n"
"v_mfma_f32_16x16x32_bf16 v[124:127], a[104:107], v[136:139], v[124:127]\n"
"v_mfma_f32_16x16x32_bf16 v[112:115], a[96:99], v[136:139], v[112:115]\n"
"buffer_load_dwordx4 v6, s[12:15], s75 offen offset:3168  lds\n"
"s_add_u32 m0, s54, 38016\n"
"s_addk_i32 s75, 0x4000\n"
"v_mfma_f32_16x16x32_bf16 v[112:115], a[108:111], v[140:143], v[112:115]\n"
"ds_write_b128 v7, v[100:103] offset:4096\n"
"v_mfma_f32_16x16x32_bf16 v[116:119], a[112:115], v[140:143], v[116:119]\n"
"s_bfe_u32 s77, s49, 0x40004\n"
"s_mul_i32 s77, s77, 36864\n"
"s_add_u32 s77, s77, s51\n"
"s_addk_i32 s77, 3072\n"
"v_cvt_pk_bf16_f32 v32, v80, v81\n"
"v_mfma_f32_16x16x32_bf16 v[116:119], a[100:103], v[136:139], v[116:119]\n"
"v_cvt_pk_bf16_f32 v33, v82, v83\n"
"s_waitcnt lgkmcnt(1)\n"
"v_mfma_f32_16x16x32_bf16 v[104:107], a[116:119], v[144:147], v[104:107]\n"
"ds_read_b128 v[128:131], v14 offset:256\n"
"v_cvt_pk_bf16_f32 v34, v84, v85\n"
"v_mfma_f32_16x16x32_bf16 v[104:107], a[128:131], v[148:151], v[104:107]\n"
"ds_read_b128 v[132:135], v15 offset:320\n"
"v_cvt_pk_bf16_f32 v35, v86, v87\n"
"v_mfma_f32_16x16x32_bf16 v[108:111], a[132:135], v[148:151], v[108:111]\n"
"ds_read_b128 v[136:139], v16 offset:256\n"
"buffer_load_dwordx4 v3, s[12:15], s75 offen offset:0  lds\n"
"v_mfma_f32_16x16x32_bf16 v[108:111], a[120:123], v[144:147], v[108:111]\n"
"ds_read_b128 v[140:143], v17 offset:320\n"
"buffer_store_dwordx4 v[32:35], v2, s[20:23], s77 offen offset:0 sc1\n"
"v_mfma_f32_16x16x32_bf16 v[120:123], a[124:127], v[144:147], v[120:123]\n"
"v_cvt_pk_bf16_f32 v36, v88, v89\n"
"v_mfma_f32_16x16x32_bf16 v[124:127], a[124:127], v[152:155], v[124:127]\n"
"v_cvt_pk_bf16_f32 v37, v90, v91\n"
"v_cvt_pk_bf16_f32 v38, v92, v93\n"
"v_mfma_f32_16x16x32_bf16 v[112:115], a[116:119], v[152:155], v[112:115]\n"
"v_cvt_pk_bf16_f32 v39, v94, v95\n"
"v_mfma_f32_16x16x32_bf16 v[112:115], a[128:131], v[156:159], v[112:115]\n"
"v_mfma_f32_16x16x32_bf16 v[116:119], a[132:135], v[156:159], v[116:119]\n"
"buffer_load_dwordx4 v4, s[12:15], s75 offen offset:1056  lds\n"
"s_waitcnt lgkmcnt(4)\n"
"s_barrier\n"
"ds_read_b128 v[100:103], v8 offset:4096\n"
"v_mfma_f32_16x16x32_bf16 v[116:119], a[120:123], v[152:155], v[116:119]\n"
"s_waitcnt lgkmcnt(1)\n"
"v_mfma_f32_16x16x32_bf16 v[104:107], a[136:139], v[128:131], v[104:107]\n"
"ds_read_b128 v[144:147], v14 offset:384\n"
"v_mfma_f32_16x16x32_bf16 v[104:107], a[148:151], v[132:135], v[104:107]\n"
"ds_read_b128 v[148:151], v15 offset:448\n"
"v_mfma_f32_16x16x32_bf16 v[108:111], a[152:155], v[132:135], v[108:111]\n"
"ds_read_b128 v[152:155], v16 offset:384\n"
"v_mfma_f32_16x16x32_bf16 v[108:111], a[140:143], v[128:131], v[108:111]\n"
"ds_read_b128 v[156:159], v17 offset:448\n"
"v_mfma_f32_16x16x32_bf16 v[120:123], a[144:147], v[128:131], v[120:123]\n"
"buffer_load_dwordx4 v5, s[12:15], s75 offen offset:2112  lds\n"
"v_mfma_f32_16x16x32_bf16 v[124:127], a[144:147], v[136:139], v[124:127]\n"
"v_mfma_f32_16x16x32_bf16 v[112:115], a[136:139], v[136:139], v[112:115]\n"
"s_waitcnt lgkmcnt(4)\n"
"v_add_f32 v96, v96, v100\n"
"v_add_f32 v97, v97, v101\n"
"v_mfma_f32_16x16x32_bf16 v[112:115], a[148:151], v[140:143], v[112:115]\n"
"buffer_store_dwordx4 v[36:39], v2, s[20:23], s77 offen offset:1024 sc1\n"
"v_add_f32 v98, v98, v102\n"
"v_mfma_f32_16x16x32_bf16 v[116:119], a[152:155], v[140:143], v[116:119]\n"
"v_add_f32 v99, v99, v103\n"
"v_cvt_pk_bf16_f32 v40, v96, v97\n"
"v_mfma_f32_16x16x32_bf16 v[116:119], a[140:143], v[136:139], v[116:119]\n"
"v_cvt_pk_bf16_f32 v41, v98, v99\n"
"s_waitcnt lgkmcnt(0)\n"
"v_mfma_f32_16x16x32_bf16 v[104:107], a[156:159], v[144:147], v[104:107]\n"
"ds_read_b128 v[128:131], v14 offset:512\n"
"buffer_load_dwordx4 v6, s[12:15], s75 offen offset:3168  lds\n"
"v_mfma_f32_16x16x32_bf16 v[104:107], a[168:171], v[148:151], v[104:107]\n"
"ds_read_b128 v[132:135], v15 offset:576\n"
"v_add3_u32 v42, v32, v33, v34\n"
"v_mfma_f32_16x16x32_bf16 v[108:111], a[172:175], v[148:151], v[108:111]\n"
"ds_read_b128 v[136:139], v16 offset:512\n"
"v_add3_u32 v42, v42, v35, v36\n"
"v_mfma_f32_16x16x32_bf16 v[108:111], a[160:163], v[144:147], v[108:111]\n"
"ds_read_b128 v[140:143], v17 offset:576\n"
"v_add3_u32 v42, v42, v37, v38\n"
"v_mfma_f32_16x16x32_bf16 v[120:123], a[164:167], v[144:147], v[120:123]\n"
"v_add3_u32 v42, v42, v39, v40\n"
"v_add3_u32 v42, v42, v41, s47\n"
"v_mfma_f32_16x16x32_bf16 v[124:127], a[164:167], v[152:155], v[124:127]\n"
"v_mfma_f32_16x16x32_bf16 v[112:115], a[156:159], v[152:155], v[112:115]\n"
"v_mfma_f32_16x16x32_bf16 v[112:115], a[168:171], v[156:159], v[112:115]\n"
"v_mfma_f32_16x16x32_bf16 v[116:119], a[172:175], v[156:159], v[116:119]\n"
"v_mfma_f32_16x16x32_bf16 v[116:119], a[160:163], v[152:155], v[116:119]\n"
"s_waitcnt lgkmcnt(0)\n"
"v_mfma_f32_16x16x32_bf16 v[104:107], a[176:179], v[128:131], v[104:107]\n"
"ds_read_b128 v[144:147], v14 offset:640\n"
"buffer_store_dwordx3 v[40:42], v9, s[20:23], s77 offen offset:2048 sc1\n"
"v_mfma_f32_16x16x32_bf16 v[104:107], a[188:191], v[132:135], v[104:107]\n"
"ds_read_b128 v[148:151], v15 offset:704\n"
"v_mfma_f32_16x16x32_bf16 v[108:111], a[192:195], v[132:135], v[108:111]\n"
"ds_read_b128 v[152:155], v16 offset:640\n"
"v_mfma_f32_16x16x32_bf16 v[108:111], a[180:183], v[128:131], v[108:111]\n"
"ds_read_b128 v[156:159], v17 offset:704\n"
"v_mfma_f32_16x16x32_bf16 v[120:123], a[184:187], v[128:131], v[120:123]\n"
"v_mfma_f32_16x16x32_bf16 v[124:127], a[184:187], v[136:139], v[124:127]\n"
"v_mfma_f32_16x16x32_bf16 v[112:115], a[176:179], v[136:139], v[112:115]\n"
"v_mfma_f32_16x16x32_bf16 v[112:115], a[188:191], v[140:143], v[112:115]\n"
"v_mfma_f32_16x16x32_bf16 v[116:119], a[192:195], v[140:143], v[116:119]\n"
"v_mfma_f32_16x16x32_bf16 v[116:119], a[180:183], v[136:139], v[116:119]\n"
"s_waitcnt lgkmcnt(0)\n"
"v_mfma_f32_16x16x32_bf16 v[104:107], a[196:199], v[144:147], v[104:107]\n"
"ds_read_b128 v[128:131], v14 offset:768\n"
"v_mfma_f32_16x16x32_bf16 v[104:107], a[208:211], v[148:151], v[104:107]\n"
"ds_read_b128 v[132:135], v15 offset:832\n"
"v_mfma_f32_16x16x32_bf16 v[108:111], a[212:215], v[148:151], v[108:111]\n"
"ds_read_b128 v[136:139], v16 offset:768\n"
"v_mfma_f32_16x16x32_bf16 v[108:111], a[200:203], v[144:147], v[108:111]\n"
"ds_read_b128 v[140:143], v17 offset:832\n"
"v_mfma_f32_16x16x32_bf16 v[120:123], a[204:207], v[144:147], v[120:123]\n"
"v_mfma_f32_16x16x32_bf16 v[124:127], a[204:207], v[152:155], v[124:127]\n"
"v_mfma_f32_16x16x32_bf16 v[112:115], a[196:199], v[152:155], v[112:115]\n"
"v_mfma_f32_16x16x32_bf16 v[112:115], a[208:211], v[156:159], v[112:115]\n"
"v_mfma_f32_16x16x32_bf16 v[116:119], a[212:215], v[156:159], v[116:119]\n"
"v_mfma_f32_16x16x32_bf16 v[116:119], a[200:203], v[152:155], v[116:119]\n"
"s_waitcnt lgkmcnt(0)\n"
"v_mfma_f32_16x16x32_bf16 v[104:107], a[216:219], v[128:131], v[104:107]\n"
"ds_read_b128 v[144:147], v14 offset:896\n"
"v_mfma_f32_16x16x32_bf16 v[104:107], a[228:231], v[132:135], v[104:107]\n"
"ds_read_b128 v[148:151], v15 offset:960\n"
"v_mfma_f32_16x16x32_bf16 v[108:111], a[232:235], v[132:135], v[108:111]\n"
"ds_read_b128 v[152:155], v16 offset:896\n"
"v_mfma_f32_16x16x32_bf16 v[108:111], a[220:223], v[128:131], v[108:111]\n"
"ds_read_b128 v[156:159], v17 offset:960\n"
"v_mfma_f32_16x16x32_bf16 v[120:123], a[224:227], v[128:131], v[120:123]\n"
"v_mfma_f32_16x16x32_bf16 v[124:127], a[224:227], v[136:139], v[124:127]\n"
"v_mfma_f32_16x16x32_bf16 v[112:115], a[216:219], v[136:139], v[112:115]\n"
"v_mfma_f32_16x16x32_bf16 v[112:115], a[228:231], v[140:143], v[112:115]\n"
"v_mfma_f32_16x16x32_bf16 v[116:119], a[232:235], v[140:143], v[116:119]\n"
"v_mfma_f32_16x16x32_bf16 v[116:119], a[220:223], v[136:139], v[116:119]\n"
"s_waitcnt lgkmcnt(0)\n"
"s_waitcnt vmcnt(14)\n"
"s_barrier\n"
"v_mfma_f32_16x16x32_bf16 v[104:107], a[236:239], v[144:147], v[104:107]\n"
"ds_read_b128 v[128:131], v14 offset:33792\n"
"v_mfma_f32_16x16x32_bf16 v[104:107], a[248:251], v[148:151], v[104:107]\n"
"ds_read_b128 v[132:135], v15 offset:33856\n"
"v_mfma_f32_16x16x32_bf16 v[108:111], a[252:255], v[148:151], v[108:111]\n"
"ds_read_b128 v[136:139], v16 offset:33792\n"
"v_mfma_f32_16x16x32_bf16 v[108:111], a[240:243], v[144:147], v[108:111]\n"
"ds_read_b128 v[140:143], v17 offset:33856\n"
"v_mfma_f32_16x16x32_bf16 v[120:123], a[244:247], v[144:147], v[120:123]\n"
"v_mfma_f32_16x16x32_bf16 v[124:127], a[244:247], v[152:155], v[124:127]\n"
"v_mfma_f32_16x16x32_bf16 v[112:115], a[236:239], v[152:155], v[112:115]\n"
"v_mfma_f32_16x16x32_bf16 v[112:115], a[248:251], v[156:159], v[112:115]\n"
"s_add_u32 s60, s52, 0\n"
"buffer_load_dwordx4 v[160:163], v2, s[20:23], s60 offen offset:0 sc1\n"
"v_mfma_f32_16x16x32_bf16 v[116:119], a[252:255], v[156:159], v[116:119]\n"
"v_mfma_f32_16x16x32_bf16 v[116:119], a[240:243], v[152:155], v[116:119]\n"
"s_waitcnt lgkmcnt(0)\n"
"v_mfma_f32_16x16x32_bf16 v[56:59], v[192:195], v[128:131], 0\n"
"ds_read_b128 v[144:147], v14 offset:33920\n"
"v_mfma_f32_16x16x32_bf16 v[56:59], v[204:207], v[132:135], v[56:59]\n"
"ds_read_b128 v[148:151], v15 offset:33984\n"
"v_mfma_f32_16x16x32_bf16 v[60:63], v[208:211], v[132:135], 0\n"
"ds_read_b128 v[152:155], v16 offset:33920\n"
"v_mfma_f32_16x16x32_bf16 v[60:63], v[196:199], v[128:131], v[60:63]\n"
"ds_read_b128 v[156:159], v17 offset:33984\n"
"v_mfma_f32_16x16x32_bf16 v[72:75], v[200:203], v[128:131], 0\n"
"v_mfma_f32_16x16x32_bf16 v[76:79], v[200:203], v[136:139], 0\n"
"v_mfma_f32_16x16x32_bf16 v[64:67], v[192:195], v[136:139], 0\n"
"ds_write_b128 v7, v[124:127] offset:8192\n"
"v_mfma_f32_16x16x32_bf16 v[64:67], v[204:207], v[140:143], v[64:67]\n"
"s_bfe_u32 s77, s49, 0x40008\n"
"s_mul_i32 s77, s77, 36864\n"
"s_add_u32 s77, s77, s51\n"
"s_addk_i32 s77, 6144\n"
"v_cvt_pk_bf16_f32 v32, v104, v105\n"
"v_mfma_f32_16x16x32_bf16 v[68:71], v[208:211], v[140:143], 0\n"
"v_cvt_pk_bf16_f32 v33, v106, v107\n"
"v_cvt_pk_bf16_f32 v34, v108, v109\n"
"v_mfma_f32_16x16x32_bf16 v[68:71], v[196:199], v[136:139], v[68:71]\n"
"v_cvt_pk_bf16_f32 v35, v110, v111\n"
"s_waitcnt lgkmcnt(1)\n"
"v_mfma_f32_16x16x32_bf16 v[56:59], v[212:215], v[144:147], v[56:59]\n"
"ds_read_b128 v[128:131], v14 offset:34048\n"
"buffer_load_dwordx4 v[164:167], v2, s[20:23], s60 offen offset:1024 sc1\n"
"v_mfma_f32_16x16x32_bf16 v[56:59], v[224:227], v[148:151], v[56:59]\n"
"ds_read_b128 v[132:135], v15 offset:34112\n"
"buffer_store_dwordx4 v[32:35], v2, s[20:23], s77 offen offset:0 sc1\n"
"v_mfma_f32_16x16x32_bf16 v[60:63], v[228:231], v[148:151], v[60:63]\n"
"ds_read_b128 v[136:139], v16 offset:34048\n"
"v_mfma_f32_16x16x32_bf16 v[60:63], v[216:219], v[144:147], v[60:63]\n"
"ds_read_b128 v[140:143], v17 offset:34112\n"
"v_cvt_pk_bf16_f32 v36, v112, v113\n"
"v_mfma_f32_16x16x32_bf16 v[72:75], v[220:223], v[144:147], v[72:75]\n"
"v_cvt_pk_bf16_f32 v37, v114, v115\n"
"v_cvt_pk_bf16_f32 v38, v116, v117\n"
"v_mfma_f32_16x16x32_bf16 v[76:79], v[220:223], v[152:155], v[76:79]\n"
"v_cvt_pk_bf16_f32 v39, v118, v119\n"
"v_mfma_f32_16x16x32_bf16 v[64:67], v[212:215], v[152:155], v[64:67]\n"
"v_mfma_f32_16x16x32_bf16 v[64:67], v[224:227], v[156:159], v[64:67]\n"
"s_waitcnt lgkmcnt(4)\n"
"s_barrier\n"
"ds_read_b128 v[124:127], v8 offset:8192\n"
"v_mfma_f32_16x16x32_bf16 v[68:71], v[228:231], v[156:159], v[68:71]\n"
"v_mfma_f32_16x16x32_bf16 v[68:71], v[216:219], v[152:155], v[68:71]\n"
"buffer_store_dwordx4 v[36:39], v2, s[20:23], s77 offen offset:1024 sc1\n"
"s_waitcnt lgkmcnt(1)\n"
"v_mfma_f32_16x16x32_bf16 v[56:59], v[232:235], v[128:131], v[56:59]\n"
"ds_read_b128 v[144:147], v14 offset:34176\n"
"v_mfma_f32_16x16x32_bf16 v[56:59], v[244:247], v[132:135], v[56:59]\n"
"ds_read_b128 v[148:151], v15 offset:34240\n"
"v_mfma_f32_16x16x32_bf16 v[60:63], v[248:251], v[132:135], v[60:63]\n"
"ds_read_b128 v[152:155], v16 offset:34176\n"
"v_mfma_f32_16x16x32_bf16 v[60:63], v[236:239], v[128:131], v[60:63]\n"
"ds_read_b128 v[156:159], v17 offset:34240\n"
"buffer_load_dwordx3 v[168:170], v9, s[20:23], s60 offen offset:2048 sc1\n"
"v_mfma_f32_16x16x32_bf16 v[72:75], v[240:243], v[128:131], v[72:75]\n"
"v_mfma_f32_16x16x32_bf16 v[76:79], v[240:243], v[136:139], v[76:79]\n"
"s_waitcnt lgkmcnt(4)\n"
"v_add_f32 v120, v120, v124\n"
"v_mfma_f32_16x16x32_bf16 v[64:67], v[232:235], v[136:139], v[64:67]\n"
"v_add_f32 v121, v121, v125\n"
"v_mfma_f32_16x16x32_bf16 v[64:67], v[244:247], v[140:143], v[64:67]\n"
"v_add_f32 v122, v122, v126\n"
"v_mfma_f32_16x16x32_bf16 v[68:71], v[248:251], v[140:143], v[68:71]\n"
"v_add_f32 v123, v123, v127\n"
"v_mfma_f32_16x16x32_bf16 v[68:71], v[236:239], v[136:139], v[68:71]\n"
"s_waitcnt lgkmcnt(0)\n"
"v_mfma_f32_16x16x32_bf16 v[56:59], v[252:255], v[144:147], v[56:59]\n"
"ds_read_b128 v[128:131], v14 offset:34304\n"
"v_mfma_f32_16x16x32_bf16 v[56:59], a[8:11], v[148:151], v[56:59]\n"
"ds_read_b128 v[132:135], v15 offset:34368\n"
"v_mfma_f32_16x16x32_bf16 v[60:63], a[12:15], v[148:151], v[60:63]\n"
"ds_read_b128 v[136:139], v16 offset:34304\n"
"v_mfma_f32_16x16x32_bf16 v[60:63], a[0:3], v[144:147], v[60:63]\n"
"ds_read_b128 v[140:143], v17 offset:34368\n"
"v_mfma_f32_16x16x32_bf16 v[72:75], a[4:7], v[144:147], v[72:75]\n"
"s_add_u32 s61, s52, 3072\n"
"buffer_load_dwordx4 v[172:175], v2, s[20:23], s61 offen offset:0 sc1\n"
"v_mfma_f32_16x16x32_bf16 v[76:79], a[4:7], v[152:155], v[76:79]\n"
"v_cvt_pk_bf16_f32 v40, v120, v121\n"
"v_mfma_f32_16x16x32_bf16 v[64:67], v[252:255], v[152:155], v[64:67]\n"
"v_cvt_pk_bf16_f32 v41, v122, v123\n"
"v_mfma_f32_16x16x32_bf16 v[64:67], a[8:11], v[156:159], v[64:67]\n"
"v_add3_u32 v42, v32, v33, v34\n"
"v_mfma_f32_16x16x32_bf16 v[68:71], a[12:15], v[156:159], v[68:71]\n"
"v_add3_u32 v42, v42, v35, v36\n"
"v_mfma_f32_16x16x32_bf16 v[68:71], a[0:3], v[152:155], v[68:71]\n"
"s_waitcnt lgkmcnt(0)\n"
"v_mfma_f32_16x16x32_bf16 v[56:59], a[16:19], v[128:131], v[56:59]\n"
"ds_read_b128 v[144:147], v14 offset:34432\n"
"v_mfma_f32_16x16x32_bf16 v[56:59], a[28:31], v[132:135], v[56:59]\n"
"ds_read_b128 v[148:151], v15 offset:34496\n"
"v_mfma_f32_16x16x32_bf16 v[60:63], a[32:35], v[132:135], v[60:63]\n"
"ds_read_b128 v[152:155], v16 offset:34432\n"
"v_mfma_f32_16x16x32_bf16 v[60:63], a[20:23], v[128:131], v[60:63]\n"
"ds_read_b128 v[156:159], v17 offset:34496\n"
"v_mfma_f32_16x16x32_bf16 v[72:75], a[24:27], v[128:131], v[72:75]\n"
"v_add3_u32 v42, v42, v37, v38\n"
"v_mfma_f32_16x16x32_bf16 v[76:79], a[24:27], v[136:139], v[76:79]\n"
"v_add3_u32 v42, v42, v39, v40\n"
"v_mfma_f32_16x16x32_bf16 v[64:67], a[16:19], v[136:139], v[64:67]\n"
"v_add3_u32 v42, v42, v41, s47\n"
"v_mfma_f32_16x16x32_bf16 v[64:67], a[28:31], v[140:143], v[64:67]\n"
"buffer_load_dwordx4 v[176:179], v2, s[20:23], s61 offen offset:1024 sc1\n"
"v_mfma_f32_16x16x32_bf16 v[68:71], a[32:35], v[140:143], v[68:71]\n"
"v_mfma_f32_16x16x32_bf16 v[68:71], a[20:23], v[136:139], v[68:71]\n"
"buffer_store_dwordx3 v[40:42], v9, s[20:23], s77 offen offset:2048 sc1\n"
"s_waitcnt lgkmcnt(0)\n"
"v_mfma_f32_16x16x32_bf16 v[56:59], a[36:39], v[144:147], v[56:59]\n"
"ds_read_b128 v[128:131], v14 offset:34560\n"
"s_waitcnt vmcnt(19)\n"
"v_cmp_ne_u32 vcc, s47, v47\n"
"s_nop 0\n"
"s_and_b64 vcc, vcc, s[6:7]\n"
"s_cbranch_vccnz Lretry7_%=\n"
"Lback7_%=:\n"
"v_mfma_f32_16x16x32_bf16 v[56:59], a[48:51], v[148:151], v[56:59]\n"
"ds_read_b128 v[132:135], v15 offset:34624\n"
"s_mov_b64 exec, s[6:7]\n"
"ds_write_b32 v53, v46\n"
"s_mov_b64 exec, -1\n"
"v_mfma_f32_16x16x32_bf16 v[60:63], a[52:55], v[148:151], v[60:63]\n"
"ds_read_b128 v[136:139], v16 offset:34560\n"
"ds_read_b128 v[192:195], v18\n"
"v_mfma_f32_16x16x32_bf16 v[60:63], a[40:43], v[144:147], v[60:63]\n"
"ds_read_b128 v[140:143], v17 offset:34624\n"
"ds_read_b128 v[196:199], v18 offset:1024\n"
"v_mfma_f32_16x16x32_bf16 v[72:75], a[44:47], v[144:147], v[72:75]\n"
"ds_read_b64 v[200:201], v19 offset:2048\n"
"v_lshlrev_b32 v212, 16, v20\n"
"v_mfma_f32_16x16x32_bf16 v[76:79], a[44:47], v[152:155], v[76:79]\n"
"v_and_b32 v213, s4, v20\n"
"v_lshlrev_b32 v214, 16, v21\n"
"v_mfma_f32_16x16x32_bf16 v[64:67], a[36:39], v[152:155], v[64:67]\n"
"v_and_b32 v215, s4, v21\n"
"v_lshlrev_b32 v216, 16, v22\n"
"v_mfma_f32_16x16x32_bf16 v[64:67], a[48:51], v[156:159], v[64:67]\n"
"v_and_b32 v217, s4, v22\n"
"v_lshlrev_b32 v218, 16, v23\n"
"v_mfma_f32_16x16x32_bf16 v[68:71], a[52:55], v[156:159], v[68:71]\n"
"v_and_b32 v219, s4, v23\n"
"v_lshlrev_b32 v220, 16, v24\n"
"v_mfma_f32_16x16x32_bf16 v[68:71], a[40:43], v[152:155], v[68:71]\n"
"v_and_b32 v221, s4, v24\n"
"s_waitcnt lgkmcnt(2)\n"
"v_mfma_f32_16x16x32_bf16 v[56:59], a[56:59], v[128:131], v[56:59]\n"
"ds_read_b128 v[144:147], v14 offset:34688\n"
"buffer_load_dwordx3 v[180:182], v9, s[20:23], s61 offen offset:2048 sc1\n"
"v_mfma_f32_16x16x32_bf16 v[56:59], a[68:71], v[132:135], v[56:59]\n"
"ds_read_b128 v[148:151], v15 offset:34752\n"
"v_lshlrev_b32 v222, 16, v25\n"
"v_mfma_f32_16x16x32_bf16 v[60:63], a[72:75], v[132:135], v[60:63]\n"
"ds_read_b128 v[152:155], v16 offset:34688\n"
"v_and_b32 v223, s4, v25\n"
"v_mfma_f32_16x16x32_bf16 v[60:63], a[60:63], v[128:131], v[60:63]\n"
"ds_read_b128 v[156:159], v17 offset:34752\n"
"v_lshlrev_b32 v224, 16, v26\n"
"v_mfma_f32_16x16x32_bf16 v[72:75], a[64:67], v[128:131], v[72:75]\n"
"v_and_b32 v225, s4, v26\n"
"v_lshlrev_b32 v226, 16, v27\n"
"v_mfma_f32_16x16x32_bf16 v[76:79], a[64:67], v[136:139], v[76:79]\n"
"v_and_b32 v227, s4, v27\n"
"v_lshlrev_b32 v228, 16, v28\n"
"v_mfma_f32_16x16x32_bf16 v[64:67], a[56:59], v[136:139], v[64:67]\n"
"v_and_b32 v229, s4, v28\n"
"v_lshlrev_b32 v230, 16, v29\n"
"v_mfma_f32_16x16x32_bf16 v[64:67], a[68:71], v[140:143], v[64:67]\n"
"v_and_b32 v231, s4, v29\n"
"s_waitcnt lgkmcnt(4)\n"
"v_lshlrev_b32 v232, 16, v192\n"
"v_mfma_f32_16x16x32_bf16 v[68:71], a[72:75], v[140:143], v[68:71]\n"
"v_and_b32 v233, s4, v192\n"
"v_lshlrev_b32 v234, 16, v193\n"
"v_mfma_f32_16x16x32_bf16 v[68:71], a[60:63], v[136:139], v[68:71]\n"
"v_and_b32 v235, s4, v193\n"
"s_waitcnt lgkmcnt(0)\n"
"s_waitcnt vmcnt(10)\n"
"s_barrier\n"
"v_mfma_f32_16x16x32_bf16 v[56:59], a[76:79], v[144:147], v[56:59]\n"
"ds_read_b128 v[128:131], v10 offset:33792\n"
"v_lshlrev_b32 v236, 16, v194\n"
"v_mfma_f32_16x16x32_bf16 v[56:59], a[88:91], v[148:151], v[56:59]\n"
"ds_read_b128 v[132:135], v11 offset:33856\n"
"v_and_b32 v237, s4, v194\n"
"v_mfma_f32_16x16x32_bf16 v[60:63], a[92:95], v[148:151], v[60:63]\n"
"ds_read_b128 v[136:139], v12 offset:33792\n"
"v_lshlrev_b32 v238, 16, v195\n"
"v_mfma_f32_16x16x32_bf16 v[60:63], a[80:83], v[144:147], v[60:63]\n"
"ds_read_b128 v[140:143], v13 offset:33856\n"
"v_and_b32 v239, s4, v195\n"
"v_mfma_f32_16x16x32_bf16 v[72:75], a[84:87], v[144:147], v[72:75]\n"
"v_lshlrev_b32 v240, 16, v196\n"
"v_and_b32 v241, s4, v196\n"
"v_mfma_f32_16x16x32_bf16 v[76:79], a[84:87], v[152:155], v[76:79]\n"
"v_lshlrev_b32 v242, 16, v197\n"
"v_and_b32 v243, s4, v197\n"
"v_mfma_f32_16x16x32_bf16 v[64:67], a[76:79], v[152:155], v[64:67]\n"
"v_lshlrev_b32 v244, 16, v198\n"
"v_and_b32 v245, s4, v198\n"
"v_mfma_f32_16x16x32_bf16 v[64:67], a[88:91], v[156:159], v[64:67]\n"
"v_lshlrev_b32 v246, 16, v199\n"
"v_and_b32 v247, s4, v199\n"
"v_mfma_f32_16x16x32_bf16 v[68:71], a[92:95], v[156:159], v[68:71]\n"
"v_lshlrev_b32 v248, 16, v200\n"
"v_and_b32 v249, s4, v200\n"
"v_mfma_f32_16x16x32_bf16 v[68:71], a[80:83], v[152:155], v[68:71]\n"
"v_lshlrev_b32 v250, 16, v201\n"
"s_waitcnt lgkmcnt(0)\n"
"v_mfma_f32_16x16x32_bf16 v[56:59], a[96:99], v[128:131], v[56:59]\n"
"ds_read_b128 v[144:147], v10 offset:33920\n"
"v_and_b32 v251, s4, v201\n"
"v_mfma_f32_16x16x32_bf16 v[56:59], a[108:111], v[132:135], v[56:59]\n"
"ds_read_b128 v[148:151], v11 offset:33984\n"
"s_lshl2_add_u32 s66, s78, s40\n"
"s_mulk_i32 s66, 10240\n"
"v_mfma_f32_16x16x32_bf16 v[60:63], a[112:115], v[132:135], v[60:63]\n"
"ds_read_b128 v[152:155], v12 offset:33920\n"
"s_mul_i32 s65, s44, 320\n"
"s_add_u32 s65, s65, s66\n"
"v_mfma_f32_16x16x32_bf16 v[60:63], a[100:103], v[128:131], v[60:63]\n"
"ds_read_b128 v[156:159], v13 offset:33984\n"
"s_mul_i32 s64, s40, 2688\n"
"s_add_u32 s64, s64, 151552\n"
"v_mfma_f32_16x16x32_bf16 v[72:75], a[104:107], v[128:131], v[72:75]\n"
"v_add_u32 v52, 0, v1\n"
"v_mul_u32_u24 v53, 3277, v52\n"
"v_mfma_f32_16x16x32_bf16 v[76:79], a[104:107], v[136:139], v[76:79]\n"
"v_lshrrev_b32 v53, 16, v53\n"
"v_add_u32 v46, v52, v53\n"
"v_mfma_f32_16x16x32_bf16 v[64:67], a[96:99], v[136:139], v[64:67]\n"
"v_lshl_add_u32 v46, v46, 4, s64\n"
"v_mul_u32_u24 v53, 60, v53\n"
"v_mfma_f32_16x16x32_bf16 v[64:67], a[108:111], v[140:143], v[64:67]\n"
"v_add_u32 v52, v52, v53\n"
"v_lshl_add_u32 v49, v52, 4, s65\n"
"v_mfma_f32_16x16x32_bf16 v[68:71], a[112:115], v[140:143], v[68:71]\n"
"v_add_u32 v52, 64, v1\n"
"v_mul_u32_u24 v53, 3277, v52\n"
"v_mfma_f32_16x16x32_bf16 v[68:71], a[100:103], v[136:139], v[68:71]\n"
"v_lshrrev_b32 v53, 16, v53\n"
"s_waitcnt lgkmcnt(0)\n"
"v_mfma_f32_16x16x32_bf16 v[56:59], a[116:119], v[144:147], v[56:59]\n"
"ds_read_b128 v[128:131], v10 offset:34048\n"
"v_add_u32 v47, v52, v53\n"
"v_mfma_f32_16x16x32_bf16 v[56:59], a[128:131], v[148:151], v[56:59]\n"
"ds_read_b128 v[132:135], v11 offset:34112\n"
"v_lshl_add_u32 v47, v47, 4, s64\n"
"v_mfma_f32_16x16x32_bf16 v[60:63], a[132:135], v[148:151], v[60:63]\n"
"ds_read_b128 v[136:139], v12 offset:34048\n"
"s_add_u32 s62, s52, 6144\n"
"buffer_load_dwordx4 v[104:107], v2, s[20:23], s62 offen offset:0 sc1\n"
"v_mfma_f32_16x16x32_bf16 v[60:63], a[120:123], v[144:147], v[60:63]\n"
"ds_read_b128 v[140:143], v13 offset:34112\n"
"v_mul_u32_u24 v53, 60, v53\n"
"v_mfma_f32_16x16x32_bf16 v[72:75], a[124:127], v[144:147], v[72:75]\n"
"v_add_u32 v52, v52, v53\n"
"v_lshl_add_u32 v50, v52, 4, s65\n"
"v_mfma_f32_16x16x32_bf16 v[76:79], a[124:127], v[152:155], v[76:79]\n"
"v_add_u32 v52, 128, v1\n"
"v_mul_u32_u24 v53, 3277, v52\n"
"v_mfma_f32_16x16x32_bf16 v[64:67], a[116:119], v[152:155], v[64:67]\n"
"v_lshrrev_b32 v53, 16, v53\n"
"v_add_u32 v48, v52, v53\n"
"v_mfma_f32_16x16x32_bf16 v[64:67], a[128:131], v[156:159], v[64:67]\n"
"v_lshl_add_u32 v48, v48, 4, s64\n"
"v_mul_u32_u24 v53, 60, v53\n"
"v_mfma_f32_16x16x32_bf16 v[68:71], a[132:135], v[156:159], v[68:71]\n"
"v_add_u32 v52, v52, v53\n"
"v_lshl_add_u32 v51, v52, 4, s65\n"
"v_mfma_f32_16x16x32_bf16 v[68:71], a[120:123], v[152:155], v[68:71]\n"
"buffer_load_dwordx4 v[108:111], v2, s[20:23], s62 offen offset:1024 sc1\n"
"s_waitcnt lgkmcnt(0)\n"
"v_mfma_f32_16x16x32_bf16 v[56:59], a[136:139], v[128:131], v[56:59]\n"
"ds_read_b128 v[144:147], v10 offset:34176\n"
"v_cmp_lt_u32 vcc, 31, v1\n"
"v_mov_b32 v52, 1073741824\n"
"s_nop 0\n"
"v_cndmask_b32 v51, v51, v52, vcc\n"
"v_mfma_f32_16x16x32_bf16 v[56:59], a[148:151], v[132:135], v[56:59]\n"
"ds_read_b128 v[148:151], v11 offset:34240\n"
"v_mfma_f32_16x16x32_bf16 v[60:63], a[152:155], v[132:135], v[60:63]\n"
"ds_read_b128 v[152:155], v12 offset:34176\n"
"v_mfma_f32_16x16x32_bf16 v[60:63], a[140:143], v[128:131], v[60:63]\n"
"ds_read_b128 v[156:159], v13 offset:34240\n"
"s_waitcnt vmcnt(6)\n"
"v_add3_u32 v44, v160, v161, v162\n"
"v_mfma_f32_16x16x32_bf16 v[72:75], a[144:147], v[128:131], v[72:75]\n"
"v_add3_u32 v44, v44, v163, v164\n"
"v_add3_u32 v44, v44, v165, v166\n"
"v_mfma_f32_16x16x32_bf16 v[76:79], a[144:147], v[136:139], v[76:79]\n"
"v_add3_u32 v44, v44, v167, v168\n"
"v_add3_u32 v44, v44, v169, s47\n"
"v_mfma_f32_16x16x32_bf16 v[64:67], a[136:139], v[136:139], v[64:67]\n"
"v_cmp_ne_u32 vcc, v44, v170\n"
"s_cbranch_vccnz Lretry8_%=\n"
"Lback8_%=:\n"
"v_lshlrev_b32 v80, 16, v160\n"
"v_mfma_f32_16x16x32_bf16 v[64:67], a[148:151], v[140:143], v[64:67]\n"
"v_and_b32 v81, s4, v160\n"
"v_lshlrev_b32 v82, 16, v161\n"
"v_mfma_f32_16x16x32_bf16 v[68:71], a[152:155], v[140:143], v[68:71]\n"
"v_and_b32 v83, s4, v161\n"
"v_lshlrev_b32 v84, 16, v162\n"
"v_mfma_f32_16x16x32_bf16 v[68:71], a[140:143], v[136:139], v[68:71]\n"
"v_and_b32 v85, s4, v162\n"
"s_waitcnt lgkmcnt(0)\n"
"v_mfma_f32_16x16x32_bf16 v[56:59], a[156:159], v[144:147], v[56:59]\n"
"ds_read_b128 v[128:131], v10 offset:34304\n"
"v_lshlrev_b32 v86, 16, v163\n"
"v_mfma_f32_16x16x32_bf16 v[56:59], a[168:171], v[148:151], v[56:59]\n"
"ds_read_b128 v[132:135], v11 offset:34368\n"
"v_and_b32 v87, s4, v163\n"
"v_mfma_f32_16x16x32_bf16 v[60:63], a[172:175], v[148:151], v[60:63]\n"
"ds_read_b128 v[136:139], v12 offset:34304\n"
"v_lshlrev_b32 v88, 16, v164\n"
"v_mfma_f32_16x16x32_bf16 v[60:63], a[160:163], v[144:147], v[60:63]\n"
"ds_read_b128 v[140:143], v13 offset:34368\n"
"v_and_b32 v89, s4, v164\n"
"v_mfma_f32_16x16x32_bf16 v[72:75], a[164:167], v[144:147], v[72:75]\n"
"v_lshlrev_b32 v90, 16, v165\n"
"v_and_b32 v91, s4, v165\n"
"v_mfma_f32_16x16x32_bf16 v[76:79], a[164:167], v[152:155], v[76:79]\n"
"v_lshlrev_b32 v92, 16, v166\n"
"v_and_b32 v93, s4, v166\n"
"v_mfma_f32_16x16x32_bf16 v[64:67], a[156:159], v[152:155], v[64:67]\n"
"v_lshlrev_b32 v94, 16, v167\n"
"v_and_b32 v95, s4, v167\n"
"v_mfma_f32_16x16x32_bf16 v[64:67], a[168:171], v[156:159], v[64:67]\n"
"v_lshlrev_b32 v96, 16, v168\n"
"v_and_b32 v97, s4, v168\n"
"v_mfma_f32_16x16x32_bf16 v[68:71], a[172:175], v[156:159], v[68:71]\n"
"v_lshlrev_b32 v98, 16, v169\n"
"v_and_b32 v99, s4, v169\n"
"v_mfma_f32_16x16x32_bf16 v[68:71], a[160:163], v[152:155], v[68:71]\n"
"s_waitcnt lgkmcnt(0)\n"
"v_mfma_f32_16x16x32_bf16 v[56:59], a[176:179], v[128:131], v[56:59]\n"
"ds_read_b128 v[144:147], v10 offset:34432\n"
"s_waitcnt vmcnt(2)\n"
"v_add3_u32 v44, v172, v173, v174\n"
"v_mfma_f32_16x16x32_bf16 v[56:59], a[188:191], v[132:135], v[56:59]\n"
"ds_read_b128 v[148:151], v11 offset:34496\n"
"v_add3_u32 v44, v44, v175, v176\n"
"v_mfma_f32_16x16x32_bf16 v[60:63], a[192:195], v[132:135], v[60:63]\n"
"ds_read_b128 v[152:155], v12 offset:34432\n"
"v_add3_u32 v44, v44, v177, v178\n"
"v_mfma_f32_16x16x32_bf16 v[60:63], a[180:183], v[128:131], v[60:63]\n"
"ds_read_b128 v[156:159], v13 offset:34496\n"
"v_add3_u32 v44, v44, v179, v180\n"
"v_mfma_f32_16x16x32_bf16 v[72:75], a[184:187], v[128:131], v[72:75]\n"
"v_add3_u32 v44, v44, v181, s47\n"
"v_cmp_ne_u32 vcc, v44, v182\n"
"s_cbranch_vccnz Lretry9_%=\n"
"Lback9_%=:\n"
"v_mfma_f32_16x16x32_bf16 v[76:79], a[184:187], v[136:139], v[76:79]\n"
"v_lshlrev_b32 v44, 16, v172\n"
"v_add_f32 v80, v80, v44\n"
"v_mfma_f32_16x16x32_bf16 v[64:67], a[176:179], v[136:139], v[64:67]\n"
"v_and_b32 v45, s4, v172\n"
"v_add_f32 v81, v81, v45\n"
"v_mfma_f32_16x16x32_bf16 v[64:67], a[188:191], v[140:143], v[64:67]\n"
"v_lshlrev_b32 v44, 16, v173\n"
"v_add_f32 v82, v82, v44\n"
"v_mfma_f32_16x16x32_bf16 v[68:71], a[192:195], v[140:143], v[68:71]\n"
"v_and_b32 v45, s4, v173\n"
"v_add_f32 v83, v83, v45\n"
"v_mfma_f32_16x16x32_bf16 v[68:71], a[180:183], v[136:139], v[68:71]\n"
"buffer_load_dwordx3 v[112:114], v9, s[20:23], s62 offen offset:2048 sc1\n"
"s_waitcnt lgkmcnt(0)\n"
"v_mfma_f32_16x16x32_bf16 v[56:59], a[196:199], v[144:147], v[56:59]\n"
"ds_read_b128 v[128:131], v10 offset:34560\n"
"v_lshlrev_b32 v44, 16, v174\n"
"v_mfma_f32_16x16x32_bf16 v[56:59], a[208:211], v[148:151], v[56:59]\n"
"ds_read_b128 v[132:135], v11 offset:34624\n"
"v_add_f32 v84, v84, v44\n"
"v_mfma_f32_16x16x32_bf16 v[60:63], a[212:215], v[148:151], v[60:63]\n"
"ds_read_b128 v[136:139], v12 offset:34560\n"
"v_and_b32 v45, s4, v174\n"
"v_mfma_f32_16x16x32_bf16 v[60:63], a[200:203], v[144:147], v[60:63]\n"
"ds_read_b128 v[140:143], v13 offset:34624\n"
"v_add_f32 v85, v85, v45\n"
"v_mfma_f32_16x16x32_bf16 v[72:75], a[204:207], v[144:147], v[72:75]\n"
"v_lshlrev_b32 v44, 16, v175\n"
"v_add_f32 v86, v86, v44\n"
"v_mfma_f32_16x16x32_bf16 v[76:79], a[204:207], v[152:155], v[76:79]\n"
"v_and_b32 v45, s4, v175\n"
"v_add_f32 v87, v87, v45\n"
"v_mfma_f32_16x16x32_bf16 v[64:67], a[196:199], v[152:155], v[64:67]\n"
"v_lshlrev_b32 v44, 16, v176\n"
"v_add_f32 v88, v88, v44\n"
"v_mfma_f32_16x16x32_bf16 v[64:67], a[208:211], v[156:159], v[64:67]\n"
"v_and_b32 v45, s4, v176\n"
"v_add_f32 v89, v89, v45\n"
"v_mfma_f32_16x16x32_bf16 v[68:71], a[212:215], v[156:159], v[68:71]\n"
"v_lshlrev_b32 v44, 16, v177\n"
"v_add_f32 v90, v90, v44\n"
"v_mfma_f32_16x16x32_bf16 v[68:71], a[200:203], v[152:155], v[68:71]\n"
"v_and_b32 v45, s4, v177\n"
"s_waitcnt lgkmcnt(0)\n"
"v_mfma_f32_16x16x32_bf16 v[56:59], a[216:219], v[128:131], v[56:59]\n"
"ds_read_b128 v[144:147], v10 offset:34688\n"
"v_add_f32 v91, v91, v45\n"
"v_mfma_f32_16x16x32_bf16 v[56:59], a[228:231], v[132:135], v[56:59]\n"
"ds_read_b128 v[148:151], v11 offset:34752\n"
"v_lshlrev_b32 v44, 16, v178\n"
"v_mfma_f32_16x16x32_bf16 v[60:63], a[232:235], v[132:135], v[60:63]\n"
"ds_read_b128 v[152:155], v12 offset:34688\n"
"v_add_f32 v92, v92, v44\n"
"v_mfma_f32_16x16x32_bf16 v[60:63], a[220:223], v[128:131], v[60:63]\n"
"ds_read_b128 v[156:159], v13 offset:34752\n"
"v_and_b32 v45, s4, v178\n"
"v_mfma_f32_16x16x32_bf16 v[72:75], a[224:227], v[128:131], v[72:75]\n"
"v_add_f32 v93, v93, v45\n"
"v_lshlrev_b32 v44, 16, v179\n"
"v_mfma_f32_16x16x32_bf16 v[76:79], a[224:227], v[136:139], v[76:79]\n"
"v_add_f32 v94, v94, v44\n"
"v_and_b32 v45, s4, v179\n"
"v_mfma_f32_16x16x32_bf16 v[64:67], a[216:219], v[136:139], v[64:67]\n"
"v_add_f32 v95, v95, v45\n"
"v_lshlrev_b32 v44, 16, v180\n"
"v_mfma_f32_16x16x32_bf16 v[64:67], a[228:231], v[140:143], v[64:67]\n"
"v_add_f32 v96, v96, v44\n"
"v_and_b32 v45, s4, v180\n"
"v_mfma_f32_16x16x32_bf16 v[68:71], a[232:235], v[140:143], v[68:71]\n"
"v_add_f32 v97, v97, v45\n"
"v_lshlrev_b32 v44, 16, v181\n"
"v_mfma_f32_16x16x32_bf16 v[68:71], a[220:223], v[136:139], v[68:71]\n"
"v_add_f32 v98, v98, v44\n"
"s_waitcnt lgkmcnt(0)\n"
"v_mfma_f32_16x16x32_bf16 v[76:79], a[244:247], v[152:155], v[76:79]\n"
"v_and_b32 v45, s4, v181\n"
"v_add_f32 v99, v99, v45\n"
"v_mfma_f32_16x16x32_bf16 v[72:75], a[244:247], v[144:147], v[72:75]\n"
"v_mfma_f32_16x16x32_bf16 v[56:59], a[236:239], v[144:147], v[56:59]\n"
"v_mfma_f32_16x16x32_bf16 v[56:59], a[248:251], v[148:151], v[56:59]\n"
"v_mfma_f32_16x16x32_bf16 v[60:63], a[252:255], v[148:151], v[60:63]\n"
"s_nop 1\n"
"ds_write_b128 v7, v[76:79]\n"
"v_mfma_f32_16x16x32_bf16 v[60:63], a[240:243], v[144:147], v[60:63]\n"
"v_mfma_f32_16x16x32_bf16 v[64:67], a[236:239], v[152:155], v[64:67]\n"
"v_mfma_f32_16x16x32_bf16 v[64:67], a[248:251], v[156:159], v[64:67]\n"
"v_mfma_f32_16x16x32_bf16 v[68:71], a[252:255], v[156:159], v[68:71]\n"
"s_waitcnt lgkmcnt(0)\n"
"s_barrier\n"
"ds_read_b128 v[76:79], v8\n"
"v_mfma_f32_16x16x32_bf16 v[68:71], a[240:243], v[152:155], v[68:71]\n"
"v_pk_add_f32 v[80:81], v[80:81], v[56:57]\n"
"v_pk_add_f32 v[82:83], v[82:83], v[58:59]\n"
"v_pk_add_f32 v[84:85], v[84:85], v[60:61]\n"
"v_pk_add_f32 v[86:87], v[86:87], v[62:63]\n"
"v_pk_add_f32 v[88:89], v[88:89], v[64:65]\n"
"v_pk_add_f32 v[90:91], v[90:91], v[66:67]\n"
"s_nop 1\n"
"v_pk_add_f32 v[92:93], v[92:93], v[68:69]\n"
"v_pk_add_f32 v[94:95], v[94:95], v[70:71]\n"
"v_pk_add_f32 v[96:97], v[96:97], v[72:73]\n"
"v_pk_add_f32 v[98:99], v[98:99], v[74:75]\n"
"s_waitcnt lgkmcnt(0)\n"
"v_pk_add_f32 v[96:97], v[96:97], v[76:77]\n"
"v_pk_add_f32 v[98:99], v[98:99], v[78:79]\n"
"v_pk_fma_f32 v[212:213], v[80:81], v[232:233], v[212:213]\n"
"v_pk_fma_f32 v[214:215], v[82:83], v[234:235], v[214:215]\n"
"v_pk_fma_f32 v[216:217], v[84:85], v[236:237], v[216:217]\n"
"v_pk_fma_f32 v[218:219], v[86:87], v[238:239], v[218:219]\n"
"v_pk_fma_f32 v[220:221], v[88:89], v[240:241], v[220:221]\n"
"v_pk_fma_f32 v[222:223], v[90:91], v[242:243], v[222:223]\n"
"v_pk_fma_f32 v[224:225], v[92:93], v[244:245], v[224:225]\n"
"v_pk_fma_f32 v[226:227], v[94:95], v[246:247], v[226:227]\n"
"v_pk_fma_f32 v[228:229], v[96:97], v[248:249], v[228:229]\n"
"v_pk_fma_f32 v[230:231], v[98:99], v[250:251], v[230:231]\n"
"s_waitcnt vmcnt(0)\n"
"v_add3_u32 v44, v104, v105, v106\n"
"v_add3_u32 v44, v44, v107, v108\n"
"v_add3_u32 v44, v44, v109, v110\n"
"v_add3_u32 v44, v44, v111, v112\n"
"v_add3_u32 v44, v44, v113, s47\n"
"v_cmp_ne_u32 vcc, v44, v114\n"
"s_cbranch_vccnz Lretry10_%=\n"
"Lback10_%=:\n"
"v_lshlrev_b32 v128, 16, v104\n"
"v_and_b32 v129, s4, v104\n"
"v_lshlrev_b32 v130, 16, v105\n"
"v_and_b32 v131, s4, v105\n"
"v_lshlrev_b32 v132, 16, v106\n"
"v_and_b32 v133, s4, v106\n"
"v_lshlrev_b32 v134, 16, v107\n"
"v_and_b32 v135, s4, v107\n"
"v_pk_fma_f32 v[212:213], v[128:129], v[232:233], v[212:213]\n"
"v_pk_fma_f32 v[214:215], v[130:131], v[234:235], v[214:215]\n"
"v_pk_fma_f32 v[216:217], v[132:133], v[236:237], v[216:217]\n"
"v_pk_fma_f32 v[218:219], v[134:135], v[238:239], v[218:219]\n"
"v_cvt_pk_bf16_f32 v192, v212, v213\n"
"v_cvt_pk_bf16_f32 v193, v214, v215\n"
"v_cvt_pk_bf16_f32 v194, v216, v217\n"
"v_cvt_pk_bf16_f32 v195, v218, v219\n"
"ds_write_b128 v30, v[192:195]\n"
"v_lshlrev_b32 v136, 16, v108\n"
"v_and_b32 v137, s4, v108\n"
"v_lshlrev_b32 v138, 16, v109\n"
"v_and_b32 v139, s4, v109\n"
"v_lshlrev_b32 v140, 16, v110\n"
"v_and_b32 v141, s4, v110\n"
"v_lshlrev_b32 v142, 16, v111\n"
"v_and_b32 v143, s4, v111\n"
"v_pk_fma_f32 v[220:221], v[136:137], v[240:241], v[220:221]\n"
"v_pk_fma_f32 v[222:223], v[138:139], v[242:243], v[222:223]\n"
"v_pk_fma_f32 v[224:225], v[140:141], v[244:245], v[224:225]\n"
"v_pk_fma_f32 v[226:227], v[142:143], v[246:247], v[226:227]\n"
"v_cvt_pk_bf16_f32 v196, v220, v221\n"
"v_cvt_pk_bf16_f32 v197, v222, v223\n"
"v_cvt_pk_bf16_f32 v198, v224, v225\n"
"v_cvt_pk_bf16_f32 v199, v226, v227\n"
"ds_write_b128 v31, v[196:199]\n"
"v_lshlrev_b32 v144, 16, v112\n"
"v_and_b32 v145, s4, v112\n"
"v_lshlrev_b32 v146, 16, v113\n"
"v_and_b32 v147, s4, v113\n"
"v_pk_fma_f32 v[228:229], v[144:145], v[248:249], v[228:229]\n"
"v_pk_fma_f32 v[230:231], v[146:147], v[250:251], v[230:231]\n"
"v_cvt_pk_bf16_f32 v200, v228, v229\n"
"v_cvt_pk_bf16_f32 v201, v230, v231\n"
"ds_write_b64 v43, v[200:201]\n"
"s_waitcnt lgkmcnt(0)\n"
"s_barrier\n"
"ds_read_b128 v[128:131], v46\n"
"ds_read_b128 v[132:135], v47\n"
"ds_read_b128 v[136:139], v48\n"
"s_waitcnt lgkmcnt(2)\n"
"buffer_store_dwordx4 v[128:131], v49, s[32:35], 0 offen \n"
"s_waitcnt lgkmcnt(1)\n"
"buffer_store_dwordx4 v[132:135], v50, s[32:35], 0 offen \n"
"s_waitcnt lgkmcnt(0)\n"
"buffer_store_dwordx4 v[136:139], v51, s[32:35], 0 offen \n"
"s_endpgm\n"
"Lretry1_%=:\n"
"s_nop 7\n"
"s_mov_b32 s59, 0\n"
"Lspin1_%=:\n"
"buffer_load_dwordx4 v[160:163], v96, s[20:23], 0 offen sc1\n"
"s_waitcnt vmcnt(0)\n"
"v_xor_b32 v46, s47, v161\n"
"v_bitop3_b32 v46, v46, v163, s47 bitop3:0xf6\n"
"v_cmp_ne_u32 vcc, 0, v46\n"
"s_nop 4\n"
"s_cbranch_vccz Lback1_%=\n"
"s_add_u32 s59, s59, 1\n"
"s_cmp_lt_u32 s59, 0x40000\n"
"s_cbranch_scc1 Lspin1_%=\n"
"s_branch Lback1_%=\n"
"Lretry0_%=:\n"
"s_nop 7\n"
"s_mov_b32 s59, 0\n"
"Lspin0_%=:\n"
"buffer_load_dwordx4 v[200:203], v2, s[20:23], s64 offen offset:0 sc1\n"
"buffer_load_dwordx4 v[204:207], v2, s[20:23], s64 offen offset:1024 sc1\n"
"s_waitcnt vmcnt(0)\n"
"v_xor_b32 v46, s47, v201\n"
"v_bitop3_b32 v46, v46, v203, s47 bitop3:0xf6\n"
"v_bitop3_b32 v46, v46, v205, s47 bitop3:0xf6\n"
"v_bitop3_b32 v46, v46, v207, s47 bitop3:0xf6\n"
"v_cmp_ne_u32 vcc, 0, v46\n"
"s_nop 4\n"
"s_cbranch_vccz Lback0_%=\n"
"s_add_u32 s59, s59, 1\n"
"s_cmp_lt_u32 s59, 0x40000\n"
"s_cbranch_scc1 Lspin0_%=\n"
"s_branch Lback0_%=\n"
"Lrare0_%=:\n"
"s_nop 7\n"
"v_mov_b32 v44, s3\n"
"s_mov_b64 exec, 1\n"
"buffer_store_dword v44, off, s[20:23], 0 sc1\n"
"s_mov_b64 exec, -1\n"
"s_nop 4\n"
"s_branch Lrareb0_%=\n"
"Lretry2_%=:\n"
"s_nop 7\n"
"s_mov_b32 s59, 0\n"
"Lspin2_%=:\n"
"buffer_load_dwordx2 v[222:223], v117, s[20:23], 0 offen sc1\n"
"s_waitcnt vmcnt(0)\n"
"v_cmp_ne_u32 vcc, s47, v223\n"
"s_nop 4\n"
"s_cbranch_vccz Lback2_%=\n"
"s_add_u32 s59, s59, 1\n"
"s_cmp_lt_u32 s59, 0x40000\n"
"s_cbranch_scc1 Lspin2_%=\n"
"s_branch Lback2_%=\n"
"Lretry4_%=:\n"
"s_nop 7\n"
"s_mov_b32 s59, 0\n"
"Lspin4_%=:\n"
"buffer_load_dword v119, v118, s[20:23], s82 offen sc1\n"
"s_waitcnt vmcnt(0)\n"
"v_cmp_ne_u32 vcc, s47, v119\n"
"s_nop 4\n"
"s_cbranch_vccz Lback4_%=\n"
"s_add_u32 s59, s59, 1\n"
"s_cmp_lt_u32 s59, 0x40000\n"
"s_cbranch_scc1 Lspin4_%=\n"
"s_branch Lback4_%=\n"
"Lretry5_%=:\n"
"s_nop 7\n"
"s_mov_b32 s59, 0\n"
"Lspin5_%=:\n"
"buffer_load_dwordx4 v[176:179], v48, s[20:23], s81 offen offset:0 sc1\n"
"buffer_load_dwordx4 v[180:183], v48, s[20:23], s81 offen offset:1024 sc1\n"
"s_waitcnt vmcnt(0)\n"
"v_xor_b32 v46, s3, v176\n"
"v_bitop3_b32 v46, v46, v177, s3 bitop3:0xf6\n"
"v_bitop3_b32 v46, v46, v178, s3 bitop3:0xf6\n"
"v_bitop3_b32 v46, v46, v179, s3 bitop3:0xf6\n"
"v_bitop3_b32 v46, v46, v180, s3 bitop3:0xf6\n"
"v_bitop3_b32 v46, v46, v181, s3 bitop3:0xf6\n"
"v_bitop3_b32 v46, v46, v182, s3 bitop3:0xf6\n"
"v_bitop3_b32 v46, v46, v183, s3 bitop3:0xf6\n"
"v_and_b32 v46, 0xff, v46\n"
"v_cmp_ne_u32 vcc, 0, v46\n"
"s_nop 4\n"
"s_cbranch_vccz Lback5_%=\n"
"s_add_u32 s59, s59, 1\n"
"s_cmp_lt_u32 s59, 0x40000\n"
"s_cbranch_scc1 Lspin5_%=\n"
"s_branch Lback5_%=\n"
"Lretry3_%=:\n"
"s_nop 7\n"
"s_mov_b32 s59, 0\n"
"Lspin3_%=:\n"
"buffer_load_dword v51, v50, s[20:23], s83 offen sc1\n"
"s_waitcnt vmcnt(0)\n"
"v_cmp_ne_u32 vcc, s47, v51\n"
"s_nop 4\n"
"s_cbranch_vccz Lback3_%=\n"
"s_add_u32 s59, s59, 1\n"
"s_cmp_lt_u32 s59, 0x40000\n"
"s_cbranch_scc1 Lspin3_%=\n"
"s_branch Lback3_%=\n"
"Lretry6_%=:\n"
"s_nop 7\n"
"s_mov_b32 s59, 0\n"
"Lspin6_%=:\n"
"buffer_load_dwordx4 v[160:163], v2, s[20:23], s48 offen offset:0 sc1\n"
"buffer_load_dwordx4 v[164:167], v2, s[20:23], s48 offen offset:1024 sc1\n"
"buffer_load_dwordx4 v[168:171], v2, s[20:23], s48 offen offset:2048 sc1\n"
"buffer_load_dwordx4 v[172:175], v2, s[20:23], s48 offen offset:3072 sc1\n"
"s_waitcnt vmcnt(0)\n"
"v_xor_b32 v48, s47, v161\n"
"v_bitop3_b32 v48, v48, v163, s47 bitop3:0xf6\n"
"v_bitop3_b32 v48, v48, v165, s47 bitop3:0xf6\n"
"v_bitop3_b32 v48, v48, v167, s47 bitop3:0xf6\n"
"v_bitop3_b32 v48, v48, v169, s47 bitop3:0xf6\n"
"v_bitop3_b32 v48, v48, v171, s47 bitop3:0xf6\n"
"v_bitop3_b32 v48, v48, v173, s47 bitop3:0xf6\n"
"v_bitop3_b32 v48, v48, v175, s47 bitop3:0xf6\n"
"v_cmp_ne_u32 vcc, 0, v48\n"
"s_nop 0\n"
"s_and_b64 vcc, vcc, s[38:39]\n"
"s_nop 4\n"
"s_cbranch_vccz Lback6_%=\n"
"s_add_u32 s59, s59, 1\n"
"s_cmp_lt_u32 s59, 0x40000\n"
"s_cbranch_scc1 Lspin6_%=\n"
"s_branch Lback6_%=\n"
"Lretry7_%=:\n"
"s_nop 7\n"
"s_mov_b32 s59, 0\n"
"Lspin7_%=:\n"
"buffer_load_dwordx2 v[46:47], v52, s[20:23], 0 offen sc1\n"
"s_waitcnt vmcnt(0)\n"
"v_cmp_ne_u32 vcc, s47, v47\n"
"s_nop 0\n"
"s_and_b64 vcc, vcc, s[6:7]\n"
"s_nop 4\n"
"s_cbranch_vccz Lback7_%=\n"
"s_add_u32 s59, s59, 1\n"
"s_cmp_lt_u32 s59, 0x40000\n"
"s_cbranch_scc1 Lspin7_%=\n"
"s_branch Lback7_%=\n"
"Lretry8_%=:\n"
"s_nop 7\n"
"s_mov_b32 s59, 0\n"
"Lspin8_%=:\n"
"buffer_load_dwordx4 v[160:163], v2, s[20:23], s60 offen offset:0 sc1\n"
"buffer_load_dwordx4 v[164:167], v2, s[20:23], s60 offen offset:1024 sc1\n"
"buffer_load_dwordx3 v[168:170], v9, s[20:23], s60 offen offset:2048 sc1\n"
"s_waitcnt vmcnt(0)\n"
"v_add3_u32 v44, v160, v161, v162\n"
"v_add3_u32 v44, v44, v163, v164\n"
"v_add3_u32 v44, v44, v165, v166\n"
"v_add3_u32 v44, v44, v167, v168\n"
"v_add3_u32 v44, v44, v169, s47\n"
"v_cmp_ne_u32 vcc, v44, v170\n"
"s_nop 4\n"
"s_cbranch_vccz Lback8_%=\n"
"s_add_u32 s59, s59, 1\n"
"s_cmp_lt_u32 s59, 0x40000\n"
"s_cbranch_scc1 Lspin8_%=\n"
"s_branch Lback8_%=\n"
"Lretry9_%=:\n"
"s_nop 7\n"
"s_mov_b32 s59, 0\n"
"Lspin9_%=:\n"
"buffer_load_dwordx4 v[172:175], v2, s[20:23], s61 offen offset:0 sc1\n"
"buffer_load_dwordx4 v[176:179], v2, s[20:23], s61 offen offset:1024 sc1\n"
"buffer_load_dwordx3 v[180:182], v9, s[20:23], s61 offen offset:2048 sc1\n"
"s_waitcnt vmcnt(0)\n"
"v_add3_u32 v44, v172, v173, v174\n"
"v_add3_u32 v44, v44, v175, v176\n"
"v_add3_u32 v44, v44, v177, v178\n"
"v_add3_u32 v44, v44, v179, v180\n"
"v_add3_u32 v44, v44, v181, s47\n"
"v_cmp_ne_u32 vcc, v44, v182\n"
"s_nop 4\n"
"s_cbranch_vccz Lback9_%=\n"
"s_add_u32 s59, s59, 1\n"
"s_cmp_lt_u32 s59, 0x40000\n"
"s_cbranch_scc1 Lspin9_%=\n"
"s_branch Lback9_%=\n"
"Lretry10_%=:\n"
"s_nop 7\n"
"s_mov_b32 s59, 0\n"
"Lspin10_%=:\n"
"buffer_load_dwordx4 v[104:107], v2, s[20:23], s62 offen offset:0 sc1\n"
"buffer_load_dwordx4 v[108:111], v2, s[20:23], s62 offen offset:1024 sc1\n"
"buffer_load_dwordx3 v[112:114], v9, s[20:23], s62 offen offset:2048 sc1\n"
"s_waitcnt vmcnt(0)\n"
"v_add3_u32 v44, v104, v105, v106\n"
"v_add3_u32 v44, v44, v107, v108\n"
"v_add3_u32 v44, v44, v109, v110\n"
"v_add3_u32 v44, v44, v111, v112\n"
"v_add3_u32 v44, v44, v113, s47\n"
"v_cmp_ne_u32 vcc, v44, v114\n"
"s_nop 4\n"
"s_cbranch_vccz Lback10_%=\n"
"s_add_u32 s59, s59, 1\n"
"s_cmp_lt_u32 s59, 0x40000\n"
"s_cbranch_scc1 Lspin10_%=\n"
"s_branch Lback10_%=\n"
"Lend_%=:\n"
  :
  : "{s[0:1]}"(__builtin_amdgcn_kernarg_segment_ptr()), "{s2}"(__builtin_amdgcn_workgroup_id_x()), "{v0}"(__builtin_amdgcn_workitem_id_x())
  : "memory", "vcc", "scc", "s3","s4","s5","s6","s7","s8","s9","s10","s11","s12","s13","s14","s15","s16","s17","s18","s19","s20","s21","s22","s23","s24","s25","s26","s27","s28","s29","s30","s31","s33","s34","s35","s36","s37","s38","s39","s40","s41","s42","s43","s44","s45","s46","s47","s48","s49","s50","s51","s52","s53","s54","s55","s56","s57","s58","s59","s60","s61","s62","s63","s64","s65","s66","s67","s68","s69","s70","s71","s72","s73","s74","s75","s76","s77","s78","s79","s80","s81","s82","s83",
    "v1","v2","v3","v4","v5","v6","v7","v8","v9","v10","v11","v12","v13","v14","v15","v16","v17","v18","v19","v20","v21","v22","v23","v24","v25","v26","v27","v28","v29","v30","v31","v32","v33","v34","v35","v36","v37","v38","v39","v40","v41","v42","v43","v44","v45","v46","v47","v48","v49","v50","v51","v52","v53","v54","v55","v56","v57","v58","v59","v60","v61","v62","v63","v64","v65","v66","v67","v68","v69","v70","v71","v72","v73","v74","v75","v76","v77","v78","v79","v80","v81","v82","v83","v84","v85","v86","v87","v88","v89","v90","v91","v92","v93","v94","v95","v96","v97","v98","v99","v100","v101","v102","v103","v104","v105","v106","v107","v108","v109","v110","v111","v112","v113","v114","v115","v116","v117","v118","v119","v120","v121","v122","v123","v124","v125","v126","v127","v128","v129","v130","v131","v132","v133","v134","v135","v136","v137","v138","v139","v140","v141","v142","v143","v144","v145","v146","v147","v148","v149","v150","v151","v152","v153","v154","v155","v156","v157","v158","v159","v160","v161","v162","v163","v164","v165","v166","v167","v168","v169","v170","v171","v172","v173","v174","v175","v176","v177","v178","v179","v180","v181","v182","v183","v184","v185","v186","v187","v188","v189","v190","v191","v192","v193","v194","v195","v196","v197","v198","v199","v200","v201","v202","v203","v204","v205","v206","v207","v208","v209","v210","v211","v212","v213","v214","v215","v216","v217","v218","v219","v220","v221","v222","v223","v224","v225","v226","v227","v228","v229","v230","v231","v232","v233","v234","v235","v236","v237","v238","v239","v240","v241","v242","v243","v244","v245","v246","v247","v248","v249","v250","v251","v252","v253","v254","v255",
    "a0","a1","a2","a3","a4","a5","a6","a7","a8","a9","a10","a11","a12","a13","a14","a15","a16","a17","a18","a19","a20","a21","a22","a23","a24","a25","a26","a27","a28","a29","a30","a31","a32","a33","a34","a35","a36","a37","a38","a39","a40","a41","a42","a43","a44","a45","a46","a47","a48","a49","a50","a51","a52","a53","a54","a55","a56","a57","a58","a59","a60","a61","a62","a63","a64","a65","a66","a67","a68","a69","a70","a71","a72","a73","a74","a75","a76","a77","a78","a79","a80","a81","a82","a83","a84","a85","a86","a87","a88","a89","a90","a91","a92","a93","a94","a95","a96","a97","a98","a99","a100","a101","a102","a103","a104","a105","a106","a107","a108","a109","a110","a111","a112","a113","a114","a115","a116","a117","a118","a119","a120","a121","a122","a123","a124","a125","a126","a127","a128","a129","a130","a131","a132","a133","a134","a135","a136","a137","a138","a139","a140","a141","a142","a143","a144","a145","a146","a147","a148","a149","a150","a151","a152","a153","a154","a155","a156","a157","a158","a159","a160","a161","a162","a163","a164","a165","a166","a167","a168","a169","a170","a171","a172","a173","a174","a175","a176","a177","a178","a179","a180","a181","a182","a183","a184","a185","a186","a187","a188","a189","a190","a191","a192","a193","a194","a195","a196","a197","a198","a199","a200","a201","a202","a203","a204","a205","a206","a207","a208","a209","a210","a211","a212","a213","a214","a215","a216","a217","a218","a219","a220","a221","a222","a223","a224","a225","a226","a227","a228","a229","a230","a231","a232","a233","a234","a235","a236","a237","a238","a239","a240","a241","a242","a243","a244","a245","a246","a247","a248","a249","a250","a251","a252","a253","a254","a255");
}

extern "C" void kernel_launch(void* const* d_in, const int* in_sizes, int n_in,
                              void* d_out, int out_size, void* d_ws, size_t ws_size,
                              hipStream_t stream) {
  (void)hipFuncSetAttribute((const void*)fnet_moe_kernel, hipFuncAttributeMaxDynamicSharedMemorySize, 163840);
  fnet_moe_kernel<<<256, 256, 163840, stream>>>(d_in[0], d_in[1], d_in[2], d_in[3], d_in[4], d_in[6], d_in[7], d_in[8], d_in[9], d_ws, d_out);
}
```
